```python
import torch

def _softmax(x, axis=-1):
    return torch.softmax(x, dim=axis)
def _gelu(x):
    return torch.nn.functional.gelu(x, approximate="tanh")
def _silu(x):
    return torch.nn.functional.silu(x)
def _sigmoid(x):
    return torch.sigmoid(x)
def _layernorm(x, axis=-1, eps=1e-5):
    m = x.mean(dim=axis, keepdim=True)
    v = x.var(dim=axis, keepdim=True, unbiased=False)
    return (x - m) / torch.sqrt(v + eps)
def _scatter_add(tgt, idx, src, axis=0):
    return tgt.clone().index_add_(axis, idx, src)
def _segment_sum(src, seg, n):
    out = torch.zeros((n,) + src.shape[1:], dtype=src.dtype)
    return out.index_add_(0, seg, src)
def _pad(x, pw):
    flat = tuple(v for pair in reversed(pw) for v in pair)
    return torch.nn.functional.pad(x, flat)

kernel_name = 'concat_heads__ssm_conv_s5__ssm_conv_s4d__prompt'

def setup_inputs():

    _w = (lambda a: (lambda t: t.to(torch.bfloat16) if t.is_floating_point() else t)(torch.as_tensor(a)))
    def _A_setup():
        B, S, D = 1, 2048, 576
        P = D
        Lambda_re = -0.5 * (lambda _x: torch.exp(torch.as_tensor(_x)))((lambda *s: torch.randn(*s, dtype=torch.bfloat16) * 0.2)(P))
        Lambda_im = (lambda *s: torch.randn(*s, dtype=torch.bfloat16) * 0.4)(P) * 1.0
        B_re = (lambda *s: torch.randn(*s, dtype=torch.bfloat16) * 0.2)(P, D) * (D ** -0.5)
        B_im = (lambda *s: torch.randn(*s, dtype=torch.bfloat16) * 0.2)(P, D) * (D ** -0.5)
        C_re = (lambda *s: torch.randn(*s, dtype=torch.bfloat16) * 0.2)(D, P) * (P ** -0.5)
        C_im = (lambda *s: torch.randn(*s, dtype=torch.bfloat16) * 0.2)(D, P) * (P ** -0.5)
        log_dt = (lambda *s: torch.randn(*s, dtype=torch.bfloat16) * 0.2)(P) * 0.3 - 2.0
        Dskip = (lambda *s: torch.randn(*s, dtype=torch.bfloat16) * 0.05)(D)
        return {
            "x": _w((lambda *s: torch.randn(*s, dtype=torch.bfloat16) * 0.05)(B, S, D)),
            "Lambda_re": _w(Lambda_re),
            "Lambda_im": _w(Lambda_im),
            "B_re": _w(B_re),
            "B_im": _w(B_im),
            "C_re": _w(C_re),
            "C_im": _w(C_im),
            "log_dt": _w(log_dt),
            "Dskip": _w(Dskip),
        }
    _A_in = _A_setup()

    def _B_setup():
        Bsz, L, D, N = 1, 2048, 576, 128
        return {
            "u": _w((lambda *s: torch.randn(*s, dtype=torch.bfloat16) * 0.05)(Bsz, L, D)),
            "log_A_real": _w((lambda *s: torch.randn(*s, dtype=torch.bfloat16) * 3.2)(D, N) * 0.5),
            "A_imag": _w((lambda *s: torch.randn(*s, dtype=torch.bfloat16) * 0.05)(D, N) * 3.14),
            "C_real": _w((lambda *s: torch.randn(*s, dtype=torch.bfloat16) * 0.1)(D, N) / N ** 0.5),
            "C_imag": _w((lambda *s: torch.randn(*s, dtype=torch.bfloat16) * 0.1)(D, N) / N ** 0.5),
            "log_dt": _w((lambda *s: torch.randn(*s, dtype=torch.bfloat16) * 0.05)(D) * 0.5 - 4.0),
            "D_skip": _w((lambda *s: torch.randn(*s, dtype=torch.bfloat16) * 0.05)(D)),
        }
    _B_in = _B_setup()

    return {
        "A_x": _A_in["x"],
        "A_Lambda_re": _A_in["Lambda_re"],
        "A_Lambda_im": _A_in["Lambda_im"],
        "A_B_re": _A_in["B_re"],
        "A_B_im": _A_in["B_im"],
        "A_C_re": _A_in["C_re"],
        "A_C_im": _A_in["C_im"],
        "A_log_dt": _A_in["log_dt"],
        "A_Dskip": _A_in["Dskip"],
        "B_u": _B_in["u"],
        "B_log_A_real": _B_in["log_A_real"],
        "B_A_imag": _B_in["A_imag"],
        "B_C_real": _B_in["C_real"],
        "B_C_imag": _B_in["C_imag"],
        "B_log_dt": _B_in["log_dt"],
        "B_D_skip": _B_in["D_skip"],
    }

def reference(A_x, A_Lambda_re, A_Lambda_im, A_B_re, A_B_im, A_C_re, A_C_im, A_log_dt, A_Dskip, B_u, B_log_A_real, B_A_imag, B_C_real, B_C_imag, B_log_dt, B_D_skip):
    def _A_ref(x, Lambda_re, Lambda_im, B_re, B_im, C_re, C_im, log_dt, Dskip):
        Bsz, L, H = x.shape
        Lam = Lambda_re + 1j * Lambda_im
        Bmat = B_re + 1j * B_im
        Cmat = C_re + 1j * C_im
        dt = torch.exp(log_dt)
        Lam_bar = torch.exp(dt * Lam)
        B_bar = ((Lam_bar - 1.0) / Lam)[:, None] * Bmat
        Bu = torch.einsum("bld,pd->blp", x + 0j, B_bar)
        h = torch.zeros_like(Bu[:, 0, :])
        hs = []
        for t in range(L):
            h = Lam_bar[None, :] * h + Bu[:, t, :]
            hs.append(h)
        hseq = torch.stack(hs, 1)
        y = 2.0 * torch.einsum("blp,dp->bld", hseq, Cmat).real + x * Dskip[None, None, :]
        return y

    def _B_ref(u, log_A_real, A_imag, C_real, C_imag, log_dt, D_skip):
        L = u.shape[1]
        dt = torch.exp(log_dt)
        A = -torch.exp(log_A_real) + 1j * A_imag
        C = (C_real + 1j * C_imag) * dt[:, None]
        pos = (lambda x, dt: x.to(dt))(torch.arange(L), log_dt.dtype)
        P = torch.exp((A * dt[:, None])[:, :, None] * pos[None, None, :])
        K = 2.0 * torch.einsum("dn,dnl->dl", C, P).real
        L2 = 2 * L
        u_t = (lambda x, *d: x.permute(*d))(u, 0, 2, 1)
        Kf = (lambda x, *a, **k: torch.fft.rfft(x.float(), *a, **k))(K, L2)
        Uf = (lambda x, *a, **k: torch.fft.rfft(x.float(), *a, **k))(u_t, L2)
        y = (lambda x, *a, **k: torch.fft.irfft(x, *a, **k).to(torch.bfloat16))(Uf * Kf[None, :, :], L2)[:, :, :L]
        y = (lambda x, *d: x.permute(*d))(y, 0, 2, 1)
        return y + u * D_skip[None, None, :]

    a = _A_ref(A_x, A_Lambda_re, A_Lambda_im, A_B_re, A_B_im, A_C_re, A_C_im, A_log_dt, A_Dskip)
    b = _B_ref(B_u, B_log_A_real, B_A_imag, B_C_real, B_C_imag, B_log_dt, B_D_skip)
    return torch.concatenate([a, b], axis=-1)
```

```cpp
#include <hip/hip_runtime.h>
#include <hip/hip_bf16.h>
#include <stdint.h>

typedef unsigned short u16;
constexpr int L = 2048, D = 576, P = 576, N = 128;

__device__ __forceinline__ float bf2f(u16 v) { return __uint_as_float(((unsigned)v) << 16); }
__device__ __forceinline__ float rbf(float f) { return __bfloat162float(__float2bfloat16(f)); }

__global__ void k_gemm1(const u16* x, const u16* Bre, const u16* Bim, const u16* Lre, const u16* Lim, const u16* ldt, float2* Bu) {
  int p = blockIdx.x * 64 + threadIdx.x;
  int l0 = blockIdx.y * 8;
  float ar[8], ai[8];
  for (int i = 0; i < 8; ++i) { ar[i] = 0; ai[i] = 0; }
  for (int d = 0; d < D; ++d) {
    float br = bf2f(Bre[p * D + d]), bi = bf2f(Bim[p * D + d]);
    for (int i = 0; i < 8; ++i) { float xv = bf2f(x[(l0 + i) * D + d]); ar[i] += xv * br; ai[i] += xv * bi; }
  }
  float dt = rbf(expf(bf2f(ldt[p])));
  float lr = bf2f(Lre[p]), li = bf2f(Lim[p]);
  float m = expf(dt * lr); float s, c; sincosf(dt * li, &s, &c);
  float zr = m * c, zi = m * s;
  float nr = zr - 1.0f, ni = zi;
  float den = lr * lr + li * li;
  float cr = (nr * lr + ni * li) / den, ci = (ni * lr - nr * li) / den;
  for (int i = 0; i < 8; ++i) Bu[(l0 + i) * P + p] = make_float2(cr * ar[i] - ci * ai[i], cr * ai[i] + ci * ar[i]);
}
__global__ void k_scan(const u16* Lre, const u16* Lim, const u16* ldt, float2* Bu) {
  int p = blockIdx.x * 64 + threadIdx.x;
  float dt = rbf(expf(bf2f(ldt[p])));
  float lr = bf2f(Lre[p]), li = bf2f(Lim[p]);
  float m = expf(dt * lr); float s, c; sincosf(dt * li, &s, &c);
  float zr = m * c, zi = m * s;
  float hr = 0, hi = 0;
  for (int l = 0; l < L; ++l) {
    float2 b = Bu[l * P + p];
    float nr = zr * hr - zi * hi + b.x, ni = zr * hi + zi * hr + b.y;
    hr = nr; hi = ni;
    Bu[l * P + p] = make_float2(hr, hi);
  }
}
__global__ void k_gemm2(const u16* x, const u16* Cre, const u16* Cim, const u16* Dsk, const float2* h, float* out) {
  int d = blockIdx.x * 64 + threadIdx.x;
  int l0 = blockIdx.y * 8;
  float a[8];
  for (int i = 0; i < 8; ++i) a[i] = 0;
  for (int p = 0; p < P; ++p) {
    float cr = bf2f(Cre[d * P + p]), ci = bf2f(Cim[d * P + p]);
    for (int i = 0; i < 8; ++i) { float2 hv = h[(l0 + i) * P + p]; a[i] += hv.x * cr - hv.y * ci; }
  }
  float ds = bf2f(Dsk[d]);
  for (int i = 0; i < 8; ++i) out[(l0 + i) * (2 * D) + d] = 2.0f * a[i] + rbf(bf2f(x[(l0 + i) * D + d]) * ds);
}
__global__ void k_kgen(const u16* lA, const u16* Aim, const u16* Cr, const u16* Ci, const u16* ldt, float* K) {
  int d = blockIdx.y;
  int l = blockIdx.x * 256 + threadIdx.x;
  float dt = rbf(expf(bf2f(ldt[d])));
  float pos = rbf((float)l);
  float acc = 0;
  for (int n = 0; n < N; ++n) {
    float ar = -rbf(expf(bf2f(lA[d * N + n]))) * dt, ai = bf2f(Aim[d * N + n]) * dt;
    float cr = bf2f(Cr[d * N + n]) * dt, ci = bf2f(Ci[d * N + n]) * dt;
    float m = expf(ar * pos); float s, c; sincosf(ai * pos, &s, &c);
    acc += cr * m * c - ci * m * s;
  }
  K[d * L + l] = 2.0f * acc;
}
__global__ void k_conv(const u16* u, const float* K, const u16* Dsk, float* out) {
  int d = blockIdx.x * 64 + threadIdx.x;
  int l = blockIdx.y;
  float acc = 0;
  for (int m = 0; m <= l; ++m) acc += K[d * L + (l - m)] * bf2f(u[m * D + d]);
  float y = rbf(acc);
  float sk = rbf(bf2f(u[l * D + d]) * bf2f(Dsk[d]));
  out[l * (2 * D) + D + d] = rbf(y + sk);
}

extern "C" void kernel_launch(void* const* d_in, const int* in_sizes, int n_in,
                              void* d_out, int out_size, void* d_ws, size_t ws_size,
                              hipStream_t stream) {
  const u16* x = (const u16*)d_in[0];
  const u16* Lre = (const u16*)d_in[1];
  const u16* Lim = (const u16*)d_in[2];
  const u16* Bre = (const u16*)d_in[3];
  const u16* Bim = (const u16*)d_in[4];
  const u16* Cre = (const u16*)d_in[5];
  const u16* Cim = (const u16*)d_in[6];
  const u16* ldtA = (const u16*)d_in[7];
  const u16* DskA = (const u16*)d_in[8];
  const u16* u = (const u16*)d_in[9];
  const u16* lA = (const u16*)d_in[10];
  const u16* Aim = (const u16*)d_in[11];
  const u16* Cr = (const u16*)d_in[12];
  const u16* Ci = (const u16*)d_in[13];
  const u16* ldtB = (const u16*)d_in[14];
  const u16* DskB = (const u16*)d_in[15];
  float* out = (float*)d_out;
  float2* Bu = (float2*)d_ws;
  float* K = (float*)((char*)d_ws + (size_t)L * P * 8);
  k_gemm1<<<dim3(P / 64, L / 8), 64, 0, stream>>>(x, Bre, Bim, Lre, Lim, ldtA, Bu);
  k_scan<<<P / 64, 64, 0, stream>>>(Lre, Lim, ldtA, Bu);
  k_gemm2<<<dim3(D / 64, L / 8), 64, 0, stream>>>(x, Cre, Cim, DskA, Bu, out);
  k_kgen<<<dim3(L / 256, D), 256, 0, stream>>>(lA, Aim, Cr, Ci, ldtB, K);
  k_conv<<<dim3(D / 64, L), 64, 0, stream>>>(u, K, DskB, out);
}
```

```cpp
#include <hip/hip_runtime.h>
#include <hip/hip_bf16.h>
#include <stdint.h>

typedef unsigned short u16;
typedef unsigned v4u __attribute__((ext_vector_type(4)));
typedef unsigned v2u __attribute__((ext_vector_type(2)));
typedef float f2 __attribute__((ext_vector_type(2)));
typedef float f4 __attribute__((ext_vector_type(4)));
typedef __bf16 bf2 __attribute__((ext_vector_type(2)));
typedef __bf16 bf8 __attribute__((ext_vector_type(8)));
typedef short s4 __attribute__((ext_vector_type(4)));
#define GAS __attribute__((address_space(1)))
#define LAS __attribute__((address_space(3)))
#define DI __device__ __forceinline__

constexpr int L = 2048, D = 576, NM = 128;
constexpr int TC = 80, NCH = 26, NS = 9;
constexpr int NA = NCH * NS;
constexpr int NB = 144;
constexpr int LDS_BYTES = 81920;

constexpr unsigned WS_EPOCH_A = 0;
constexpr unsigned WS_EPOCH_B = 256;
constexpr unsigned WS_FLAG_U = 4096;
constexpr unsigned WS_FLAG_Y = 4096 + 8192;
constexpr unsigned WS_E = 32768;
constexpr unsigned WS_E_BYTES = NCH * 36 * 256;
constexpr unsigned WS_H = WS_E + 262144;
constexpr unsigned WS_H_BYTES = NCH * 5 * 36 * 1024;
constexpr unsigned WS_UT = WS_H + WS_H_BYTES;
constexpr unsigned WS_UT_BYTES = 9 * 16 * 16 * 1024;
constexpr unsigned WS_YT = WS_UT + WS_UT_BYTES;
constexpr unsigned WS_YT_BYTES = 9 * 16 * 16 * 1024;
constexpr unsigned WS_TOTAL = WS_YT + WS_YT_BYTES;
#define ST(i) do {} while (0)

constexpr unsigned SPIN_CAP = 1u << 17;

struct Params {
  const u16 *x, *Lre, *Lim, *Bre, *Bim, *Cre, *Cim, *ldtA, *DskA;
  const u16 *u, *lA, *Aim, *Cr, *Ci, *ldtB, *DskB;
  float* out;
  char* ws;
};

DI float bf2f(unsigned v) { return __uint_as_float(v << 16); }
DI float bfhi(unsigned v) { return __uint_as_float(v & 0xffff0000u); }
DI unsigned pk2(float a, float b) { f2 z = {a, b}; return __builtin_bit_cast(unsigned, __builtin_convertvector(z, bf2)); }
DI float rbf(float f) { return bf2f(pk2(f, 0.f) & 0xffffu); }
DI f2 cmul(f2 a, f2 b) {
  f2 t = a * (f2){b.x, b.x};
  return __builtin_elementwise_fma((f2){a.y, a.x}, (f2){-b.y, b.y}, t);
}
DI f2 cfma(f2 a, f2 b, f2 c) {
  f2 t = __builtin_elementwise_fma(a, (f2){b.x, b.x}, c);
  return __builtin_elementwise_fma((f2){a.y, a.x}, (f2){-b.y, b.y}, t);
}
DI float fexp(float x) {
  const float c_hi = 1.44269502162933349609375f, c_lo = 1.925963033500011e-8f;
  float t = x * c_hi;
  float e = __builtin_fmaf(x, c_hi, -t);
  e = __builtin_fmaf(x, c_lo, e);
  float r = __builtin_amdgcn_exp2f(t);
  return __builtin_fmaf(r * e, 0.693147180559945f, r);
}
DI f2 cexp(float re, float im) {
  const float r_hi = 0.15915494f, r_lo = 6.4206382e-9f;
  float m = fexp(re);
  float t = im * r_hi;
  float e = __builtin_fmaf(im, r_hi, -t);
  e = __builtin_fmaf(im, r_lo, e);
  float f = __builtin_amdgcn_fractf(t) + e;
  return (f2){m * __builtin_amdgcn_cosf(f), m * __builtin_amdgcn_sinf(f)};
}
DI f4 mfma16(v4u a, v4u b, f4 c) {
  return __builtin_amdgcn_mfma_f32_16x16x32_bf16(__builtin_bit_cast(bf8, a), __builtin_bit_cast(bf8, b), c, 0, 0, 0);
}
DI v4u ldg16(const void* base, unsigned off, int cst = 0) { return *(const GAS v4u*)((const GAS char*)base + off + cst); }
DI v4u lds16(LAS char* s, unsigned off) { return *(LAS v4u*)(s + off); }
DI v2u ldstr(LAS char* s, unsigned off) {
  return __builtin_bit_cast(v2u, __builtin_amdgcn_ds_read_tr16_b64_v4i16((LAS s4*)(s + off)));
}
#define FENCE() do { __builtin_amdgcn_sched_barrier(0); asm volatile("" ::: "memory"); } while (0)
#define BARRIER() do { __builtin_amdgcn_sched_barrier(0); asm volatile("s_waitcnt lgkmcnt(0)\n\ts_barrier" ::: "memory"); __builtin_amdgcn_sched_barrier(0); } while (0)

struct QMask { unsigned long long e1, o1, e2, o2; };
#define TRC(d, s0, s1, P) "v_cndmask_b32_dpp " d ", " s0 ", " s1 ", vcc quad_perm:" P " row_mask:0xf bank_mask:0xf\n\t"
DI void tr4half(v2u& a0, v2u& a1, v2u& a2, v2u& a3, const QMask& m) {
  unsigned a00 = a0.x, a01 = a0.y, a10 = a1.x, a11 = a1.y, a20 = a2.x, a21 = a2.y, a30 = a3.x, a31 = a3.y;
  unsigned t00, t01, t20, t21;
  asm volatile(
      "s_mov_b64 vcc, %12\n\ts_nop 0\n\t"
      TRC("%8", "%2", "%0", "[1,0,3,2]") TRC("%9", "%3", "%1", "[1,0,3,2]")
      TRC("%10", "%6", "%4", "[1,0,3,2]") TRC("%11", "%7", "%5", "[1,0,3,2]")
      "s_mov_b64 vcc, %13\n\t"
      TRC("%2", "%0", "%2", "[1,0,3,2]") TRC("%3", "%1", "%3", "[1,0,3,2]")
      TRC("%6", "%4", "%6", "[1,0,3,2]") TRC("%7", "%5", "%7", "[1,0,3,2]")
      "s_mov_b64 vcc, %14\n\t"
      TRC("%0", "%10", "%8", "[2,3,0,1]") TRC("%1", "%11", "%9", "[2,3,0,1]")
      TRC("%4", "%6", "%2", "[2,3,0,1]") TRC("%5", "%7", "%3", "[2,3,0,1]")
      "s_mov_b64 vcc, %15\n\t"
      TRC("%10", "%8", "%10", "[2,3,0,1]") TRC("%11", "%9", "%11", "[2,3,0,1]")
      TRC("%6", "%2", "%6", "[2,3,0,1]") TRC("%7", "%3", "%7", "[2,3,0,1]")
      "s_nop 0"
      : "+v"(a00), "+v"(a01), "+v"(a10), "+v"(a11), "+v"(a20), "+v"(a21), "+v"(a30), "+v"(a31),
        "=&v"(t00), "=&v"(t01), "=&v"(t20), "=&v"(t21)
      : "s"(m.e1), "s"(m.o1), "s"(m.e2), "s"(m.o2)
      : "vcc");
  a0 = (v2u){a00, a01}; a1 = (v2u){a20, a21}; a2 = (v2u){t20, t21}; a3 = (v2u){a30, a31};
}
DI void tr4(v4u& a0, v4u& a1, v4u& a2, v4u& a3, const QMask& m) {
  v2u l0 = a0.xy, l1 = a1.xy, l2 = a2.xy, l3 = a3.xy, h0 = a0.zw, h1 = a1.zw, h2 = a2.zw, h3 = a3.zw;
  tr4half(l0, l1, l2, l3, m);
  tr4half(h0, h1, h2, h3, m);
  a0 = (v4u){l0.x, l0.y, h0.x, h0.y}; a1 = (v4u){l1.x, l1.y, h1.x, h1.y};
  a2 = (v4u){l2.x, l2.y, h2.x, h2.y}; a3 = (v4u){l3.x, l3.y, h3.x, h3.y};
}
DI float xsum_groups(float v) {
  auto a = __builtin_amdgcn_permlane16_swap(__float_as_uint(v), __float_as_uint(v), false, false);
  v = __uint_as_float(a[0]) + __uint_as_float(a[1]);
  auto b = __builtin_amdgcn_permlane32_swap(__float_as_uint(v), __float_as_uint(v), false, false);
  return __uint_as_float(b[0]) + __uint_as_float(b[1]);
}
DI unsigned tag2(unsigned t) { return (t * 0x9E3779B1u) ^ 0x5bd1e995u; }
DI unsigned ld_epoch(const char* ws, unsigned off) {
  return __hip_atomic_load((const GAS unsigned*)(ws + off), __ATOMIC_RELAXED, __HIP_MEMORY_SCOPE_AGENT);
}

DI void role_a(const Params& p, LAS char* sm, int bid) {
  const int tid = threadIdx.x, lane = tid & 63;
  const int w = __builtin_amdgcn_readfirstlane(tid >> 6);
  int id; { const int q = NA / 8, r = NA % 8, xcd = bid & 7, off = bid >> 3; id = (xcd < r ? xcd * (q + 1) : r * (q + 1) + (xcd - r) * q) + off; }
  const int c = id / NS, s = id % NS;
  const int i16 = lane & 15, g = lane >> 4, c0 = lane & 3, c1 = (lane >> 2) & 3;
  const QMask qm = {0x5555555555555555ull, 0xAAAAAAAAAAAAAAAAull, 0x3333333333333333ull, 0xCCCCCCCCCCCCCCCCull};
  const auto rs = __builtin_amdgcn_make_buffer_rsrc((void*)p.ws, 0, WS_TOTAL, 0x00020000);
  __builtin_amdgcn_s_setprio(2);
  ST(0);

  const unsigned ep = ld_epoch(p.ws, WS_EPOCH_A);
  const int pl = 64 * s + 8 * w + (i16 & 7) + 32 * (i16 >> 3);
  const unsigned rLre = *(const GAS u16*)(p.Lre + pl), rLim = *(const GAS u16*)(p.Lim + pl), rLdt = *(const GAS u16*)(p.ldtA + pl);
  const int kap = 4 * s + w;
  const unsigned hslot = kap < 32 ? 9 * (kap >> 3) + (kap & 7) : 9 * (kap - 32) + 8;
  const unsigned hst = WS_H + (unsigned)(c * 180 + hslot) * 1024 + lane * 16;
  {
    const v4u pz = {~0u, ~0u, ~0u, ~0u};
#pragma unroll
    for (int t = 0; t < 5; ++t) __builtin_amdgcn_raw_buffer_store_b128(pz, rs, hst, t * 36864, 16);
  }
  FENCE();
  auto kb_of = [&](int ord) { int k = s + 1 + ord; return k >= 9 ? k - 9 : k; };
  const auto rx = __builtin_amdgcn_make_buffer_rsrc((void*)p.x, 0, L * D * 2, 0x00020000);
  const auto rbr = __builtin_amdgcn_make_buffer_rsrc((void*)p.Bre, 0, D * D * 2, 0x00020000);
  const auto rbi = __builtin_amdgcn_make_buffer_rsrc((void*)p.Bim, 0, D * D * 2, 0x00020000);
  const unsigned xv0 = (unsigned)(20 * (lane >> 5) + ((lane >> 3) & 3)) * 1152u + (lane & 7) * 16u;
  auto ldx = [&](int ord, int sub) {
    return __builtin_amdgcn_raw_buffer_load_b128(rx, xv0, (c * TC + 40 * (sub & 1) + 4 * (sub >> 1)) * 1152 + kb_of(ord) * 128, 0);
  };
  const unsigned xw = (lane >> 3) * 128 + (((lane & 7) ^ (lane >> 4)) * 16);
  LAS char* xwb = sm + xw + (w >> 1) * 10240 + (w & 1) * 5120;
  const unsigned brow = (unsigned)(4 * (c1 & 1) + 32 * (c1 >> 1)) * 1152u;
  const int bso = (64 * s + 8 * w) * 1152;

  v4u xs[5][5], br[4][4], bi[4][4], bm[4];
  auto load_x = [&](int G) {
#pragma unroll
    for (int i = 0; i < 5; ++i) xs[G][i] = ldx(2 * G + (w >> 1), 5 * (w & 1) + i);
  };
  auto load_b = [&](int G) {
    const unsigned o = brow + (unsigned)(g >> 1 ? kb_of(2 * G + 1) : kb_of(2 * G)) * 128u + (4 * (g & 1) + c0) * 16u;
#pragma unroll
    for (int a = 0; a < 4; ++a) br[G][a] = __builtin_amdgcn_raw_buffer_load_b128(rbr, o + a * 1152, bso, 0);
#pragma unroll
    for (int a = 0; a < 4; ++a) bi[G][a] = __builtin_amdgcn_raw_buffer_load_b128(rbi, o + a * 1152, bso, 0);
  };
  auto store_x = [&](int G) {
#pragma unroll
    for (int i = 0; i < 5; ++i) *(LAS v4u*)(xwb + G * 20480 + i * 1024) = xs[G][i];
  };
  load_x(0); load_b(0); load_x(1); load_b(1); load_x(2); load_b(2);
  FENCE();

  const float dt = rbf(fexp(bf2f(rLdt)));
  const float lre = bf2f(rLre), lim = bf2f(rLim);
  const float ar = dt * lre, ai = dt * lim;
  const f2 lam = cexp(ar, ai);
  f2 coef2;
  { const float nr = lam.x - 1.0f, ni = lam.y, den = 2.0f / (lre * lre + lim * lim);
    coef2 = (f2){(nr * lre + ni * lim) * den, (ni * lre - nr * lim) * den}; }
  const float fe = (float)(20 * (3 - g));
  const f2 wend = cexp(ar * fe, ai * fe);
  const unsigned tagA = ep + 1u, tagB = tag2(ep + 1u);
  FENCE();

  f4 aR[5], aI[5];
#pragma unroll
  for (int t = 0; t < 5; ++t) { aR[t] = (f4){0, 0, 0, 0}; aI[t] = (f4){0, 0, 0, 0}; }
  const unsigned fsw = (i16 >> 1) & 3;
  const unsigned ard = (g >> 1) * 10240 + i16 * 128 + (g & 1) * 64;
  auto mma_group = [&](int G) {
    tr4(br[G][0], br[G][1], br[G][2], br[G][3], qm);
    tr4(bi[G][0], bi[G][1], bi[G][2], bi[G][3], qm);
#pragma unroll
    for (int k = 0; k < 4; ++k) {
      v4u xa[5];
#pragma unroll
      for (int t = 0; t < 5; ++t) xa[t] = lds16(sm, G * 20480 + t * 2048 + ard + ((k ^ fsw) * 16));
#pragma unroll
      for (int t = 0; t < 5; ++t) { aR[t] = mfma16(xa[t], br[G][k], aR[t]); aI[t] = mfma16(xa[t], bi[G][k], aI[t]); }
    }
  };
  v4u cf[2][4][4], c8[4];
  const char* cbase = (const char*)(g >> 1 ? p.Cim : p.Cre) + (unsigned)(64 * s + 4 * c1) * 1152u + (32 * (g & 1) + 8 * c0) * 2u;
  auto load_c = [&](int b, int n) {
#pragma unroll
    for (int a = 0; a < 4; ++a) cf[b][n][a] = ldg16(cbase, (unsigned)(16 * n * 1152 + 2 * w * 128), a * 1152 + b * 128);
  };

  store_x(0);
  BARRIER();
  ST(1);
  mma_group(0);
  FENCE();
  load_x(3);
  load_b(3);
  FENCE();
  store_x(1); BARRIER();
  mma_group(1);
  FENCE();
#pragma unroll
  for (int i = 0; i < 3; ++i) { int sub = w + 4 * i; sub = sub < 10 ? sub : 9; xs[4][i] = ldx(8, sub); }
  {
    const char* mb = (const char*)(c0 >> 1 ? p.Bim : p.Bre);
    const unsigned o = bso + brow + (unsigned)kb_of(8) * 128u + (4 * (g & 1) + 2 * (g >> 1) + (c0 & 1)) * 16u;
#pragma unroll
    for (int a = 0; a < 4; ++a) bm[a] = ldg16(mb, o, a * 1152);
  }
  load_c(0, 0);
  FENCE();
  store_x(2);
  BARRIER();
  mma_group(2);
  FENCE();
  load_c(0, 1); load_c(0, 2);
  FENCE();
  store_x(3);
#pragma unroll
  for (int i = 0; i < 3; ++i) { int sub = w + 4 * i; sub = sub < 10 ? sub : 9; *(LAS v4u*)(sm + sub * 1024 + xw) = xs[4][i]; }
  BARRIER();
  mma_group(3);
  FENCE();
  load_c(0, 3);
  FENCE();
  {
    tr4(bm[0], bm[1], bm[2], bm[3], qm);
    const unsigned a4 = i16 * 128;
#pragma unroll
    for (int k = 0; k < 2; ++k) {
      v4u xa[5];
#pragma unroll
      for (int t = 0; t < 5; ++t) xa[t] = lds16(sm, t * 2048 + a4 + (((4 * (g & 1) + 2 * (g >> 1) + k) ^ fsw) * 16));
#pragma unroll
      for (int t = 0; t < 5; ++t) { aR[t] = mfma16(xa[t], bm[k], aR[t]); aI[t] = mfma16(xa[t], bm[2 + k], aI[t]); }
    }
  }
  FENCE();
  ST(2);

  f2 h[20];
  h[0] = (f2){aR[0][0], aI[0][0]};
#pragma unroll
  for (int j = 1; j < 20; ++j) h[j] = cfma(h[j - 1], lam, (f2){aR[j >> 2][j & 3], aI[j >> 2][j & 3]});
  {
    const f2 e = cmul(h[19], wend);
    const float er = xsum_groups(e.x), ei = xsum_groups(e.y);
    const v4u gr = {__float_as_uint(er), tagA, __float_as_uint(ei), tagB};
    if (g == 0) __builtin_amdgcn_raw_buffer_store_b128(gr, rs, WS_E + (unsigned)(c * 36 + kap) * 256 + i16 * 16, 0, 16);
  }
  FENCE();
  ST(3);
  f2 pre;
  {
    const f2 l20 = cexp(ar * 20.f, ai * 20.f);
    auto r16 = __builtin_amdgcn_permlane16_swap(__float_as_uint(h[19].x), __float_as_uint(h[19].x), false, false);
    auto i16s = __builtin_amdgcn_permlane16_swap(__float_as_uint(h[19].y), __float_as_uint(h[19].y), false, false);
    auto r02 = __builtin_amdgcn_permlane32_swap(r16[0], r16[0], false, false);
    auto i02 = __builtin_amdgcn_permlane32_swap(i16s[0], i16s[0], false, false);
    auto r13 = __builtin_amdgcn_permlane32_swap(r16[1], r16[1], false, false);
    auto i13 = __builtin_amdgcn_permlane32_swap(i16s[1], i16s[1], false, false);
    const f2 e0 = {__uint_as_float(r02[0]), __uint_as_float(i02[0])}, e2 = {__uint_as_float(r02[1]), __uint_as_float(i02[1])};
    const f2 e1 = {__uint_as_float(r13[0]), __uint_as_float(i13[0])};
    const f2 p2 = cfma(e0, l20, e1), p3 = cfma(p2, l20, e2);
    pre = g == 0 ? (f2){0.f, 0.f} : g == 1 ? e0 : g == 2 ? p2 : p3;
  }
  f2 lt[20];
  lt[0] = cmul(coef2, lam);
#pragma unroll
  for (int j = 1; j < 20; ++j) lt[j] = cmul(lt[j - 1], lam);
#pragma unroll
  for (int j = 0; j < 20; ++j) h[j] = cmul(h[j], coef2);
  const float f0 = (float)(80 * ((c - 1 - g) & 3));
  const f2 wq = cexp(ar * f0, ai * f0), w320 = cexp(ar * 320.f, ai * 320.f);
  const float fg = (float)(20 * g);
  const f2 wg20 = cexp(ar * fg, ai * fg);
  tr4(cf[0][0][0], cf[0][0][1], cf[0][0][2], cf[0][0][3], qm);
  tr4(cf[0][1][0], cf[0][1][1], cf[0][1][2], cf[0][1][3], qm);
  tr4(cf[0][2][0], cf[0][2][1], cf[0][2][2], cf[0][2][3], qm);
  tr4(cf[0][3][0], cf[0][3][1], cf[0][3][2], cf[0][3][3], qm);
  FENCE();
  ST(4);

  f2 H = {0.f, 0.f};
  {
    const int kq = lane < 8 ? 8 * w + lane : 32 + w;
    const unsigned oq = WS_E + (unsigned)(c * 36 + kq) * 256;
    const unsigned oe = WS_E + (unsigned)(g * 36 + kap) * 256 + i16 * 16;
    v4u e[7];
#pragma unroll 1
    for (unsigned spin = 0; spin < SPIN_CAP; ++spin) {
      asm volatile("" ::: "memory");
      const v4u q = __builtin_amdgcn_raw_buffer_load_b128(rs, oq, 0, 16);
#pragma unroll
      for (int m = 0; m < 7; ++m) e[m] = __builtin_amdgcn_raw_buffer_load_b128(rs, g + 4 * m < c ? oe + m * (4 * 36 * 256) : oq, 0, 16);
      bool ok = q.y == tagA && q.w == tagB;
#pragma unroll
      for (int m = 0; m < 7; ++m) ok = ok && e[m].y == tagA && e[m].w == tagB;
      if (__all(ok)) break;
      __builtin_amdgcn_s_sleep(1);
    }
#pragma unroll
    for (int m = 0; m < 7; ++m) {
      const f2 nx = cfma(H, w320, (f2){__uint_as_float(e[m].x), __uint_as_float(e[m].z)});
      H = g + 4 * m < c ? nx : H;
    }
    H = cmul(H, wq);
    H = (f2){xsum_groups(H.x), xsum_groups(H.y)};
  }
  FENCE();
  ST(5);
  load_c(1, 0); load_c(1, 1); load_c(1, 2); load_c(1, 3);
#pragma unroll
  for (int a = 0; a < 4; ++a) c8[a] = ldg16(cbase, (unsigned)(16 * w) * 1152u, a * 1152 + 8 * 128);
  FENCE();
  {
    const f2 car = cfma(H, wg20, pre);
    LAS char* tb = sm + 10240 + w * 5376;
#pragma unroll
    for (int t = 0; t < 5; ++t) {
      f2 v[4];
#pragma unroll
      for (int r = 0; r < 4; ++r) v[r] = cfma(lt[4 * t + r], car, h[4 * t + r]);
      *(LAS v2u*)(tb + i16 * 168 + 40 * g + 8 * t) = (v2u){pk2(v[0].x, v[1].x), pk2(v[2].x, v[3].x)};
      *(LAS v2u*)(tb + (16 + i16) * 168 + 40 * g + 8 * t) = (v2u){pk2(-v[0].y, -v[1].y), pk2(-v[2].y, -v[3].y)};
    }
    asm volatile("" ::: "memory");
    const unsigned ra = (8 * g + (i16 >> 2)) * 168 + (i16 & 3) * 8;
#pragma unroll
    for (int t = 0; t < 5; ++t) {
      const v2u lo = ldstr(tb, ra + 32 * t), hi = ldstr(tb, ra + 4 * 168 + 32 * t);
      __builtin_amdgcn_raw_buffer_store_b128((v4u){lo.x, lo.y, hi.x, hi.y}, rs, hst, t * 36864, 16);
    }
  }
  FENCE();
  ST(6);
  tr4(cf[1][0][0], cf[1][0][1], cf[1][0][2], cf[1][0][3], qm);
  tr4(cf[1][1][0], cf[1][1][1], cf[1][1][2], cf[1][1][3], qm);
  tr4(cf[1][2][0], cf[1][2][1], cf[1][2][2], cf[1][2][3], qm);
  tr4(cf[1][3][0], cf[1][3][1], cf[1][3][2], cf[1][3][3], qm);
  tr4(c8[0], c8[1], c8[2], c8[3], qm);
  LAS char* c8s = sm + 32768;
#pragma unroll
  for (int a = 0; a < 4; ++a) *(LAS v4u*)(c8s + (a * 4 + w) * 1024 + lane * 16) = c8[a];
  const v2u dsk = *(const GAS v2u*)(p.DskA + 64 * s + 4 * i16);
  BARRIER();
  ST(7);

  const unsigned hld = WS_H + (unsigned)(c * 180 + 9 * w) * 1024 + lane * 16;
  v4u hf[2][9];
  auto load_h = [&](int t) {
#pragma unroll
    for (int i = 0; i < 9; ++i) hf[t & 1][i] = __builtin_amdgcn_raw_buffer_load_b128(rs, hld + (i & 3) * 1024, t * 36864 + (i >> 2) * 4096, 16);
  };
  auto bad_h = [&](int t) {
    unsigned m = 0;
#pragma unroll
    for (int i = 0; i < 9; ++i) { const v4u v = hf[t & 1][i]; m = max(max(m, v.x), v.y); m = max(max(m, v.z), v.w); }
    return __any(m == ~0u);
  };
  LAS char* red = sm + 49152;
  const float dk[4] = {bf2f(dsk.x & 0xffffu), bfhi(dsk.x), bf2f(dsk.y & 0xffffu), bfhi(dsk.y)};
  load_h(0); load_h(1);
  FENCE();
#pragma unroll
  for (int t = 0; t < 5; ++t) {
#pragma unroll 1
    for (unsigned spin = 0; spin < SPIN_CAP; ++spin) {
      if (!bad_h(t)) break;
      asm volatile("" ::: "memory");
      load_h(t);
    }
    FENCE();
    if (t == 0) ST(8);
    f4 acc[4];
#pragma unroll
    for (int n = 0; n < 4; ++n) acc[n] = mfma16(lds16(c8s, (w * 4 + n) * 1024 + lane * 16), hf[t & 1][8], (f4){0, 0, 0, 0});
#pragma unroll
    for (int i = 0; i < 8; ++i)
#pragma unroll
      for (int n = 0; n < 4; ++n) acc[n] = mfma16(cf[i >> 2][n][i & 3], hf[t & 1][i], acc[n]);
    FENCE();
    if (t + 2 < 5) load_h(t + 2);
    FENCE();
    LAS char* rb = (t & 1) ? sm + 10240 : red;
#pragma unroll
    for (int n = 0; n < 4; ++n) *(LAS f4*)(rb + w * 4352 + i16 * 272 + n * 64 + g * 16) = acc[n];
    BARRIER();
    {
      const int tr = 4 * w + g, tl = 16 * t + tr;
      f4 y = *(LAS f4*)(rb + tr * 272 + i16 * 16);
#pragma unroll
      for (int q = 1; q < 4; ++q) y += *(LAS f4*)(rb + q * 4352 + tr * 272 + i16 * 16);
      const int gq = tl / 20, rem = tl - 20 * gq, rho = 4 * gq + (rem & 3);
      const v2u xv = *(LAS v2u*)(sm + (rem >> 2) * 2048 + rho * 128 + (((i16 >> 1) ^ ((rho >> 1) & 3)) * 16) + (i16 & 1) * 8);
      const unsigned s01 = pk2(bf2f(xv.x & 0xffffu) * dk[0], bfhi(xv.x) * dk[1]), s23 = pk2(bf2f(xv.y & 0xffffu) * dk[2], bfhi(xv.y) * dk[3]);
      y += (f4){bf2f(s01 & 0xffffu), bfhi(s01), bf2f(s23 & 0xffffu), bfhi(s23)};
      const int row = c * TC + tl;
      if (row < L) *(GAS f4*)(p.out + (size_t)row * 1152 + 64 * s + 4 * i16) = y;
    }
    if (t == 0 && id == NA - NS && tid == 0)
      __hip_atomic_store((GAS unsigned*)(p.ws + WS_EPOCH_A), tagA, __ATOMIC_RELAXED, __HIP_MEMORY_SCOPE_AGENT);
    FENCE();
  }
  ST(9);
}

DI void role_b(const Params& p, LAS char* sm, int b) {
  const int tid = threadIdx.x, lane = tid & 63;
  const int w = __builtin_amdgcn_readfirstlane(tid >> 6);
  const int grp = b >> 4, r16 = b & 15, ch = 64 * grp + 4 * r16 + w;
  const int i16 = lane & 15, g = lane >> 4;
  const auto rs = __builtin_amdgcn_make_buffer_rsrc((void*)p.ws, 0, WS_TOTAL, 0x00020000);
  ST(0);
  constexpr unsigned WB = 13312;
  LAS char* kb = sm + w * WB;
  LAS char* ub = kb + 8448;
  LAS char* tile = sm + 4 * WB;

  const unsigned ep = ld_epoch(p.ws, WS_EPOCH_B + grp * 256);
  const unsigned rA = *(const GAS unsigned*)(p.lA + ch * NM + 2 * lane), rI = *(const GAS unsigned*)(p.Aim + ch * NM + 2 * lane);
  const unsigned rCr = *(const GAS unsigned*)(p.Cr + ch * NM + 2 * lane), rCi = *(const GAS unsigned*)(p.Ci + ch * NM + 2 * lane);
  const unsigned rdt = *(const GAS u16*)(p.ldtB + ch), rdk = *(const GAS u16*)(p.DskB + ch);
  v4u ut[4];
#pragma unroll
  for (int i = 0; i < 4; ++i) ut[i] = ldg16(p.u, (unsigned)(128 * r16 + 32 * w + 8 * i + (lane >> 3)) * 1152u + grp * 128u + (lane & 7) * 16u);
  FENCE();
  const unsigned tagA = ep + 1u, tagB = tag2(ep + 1u);
  if (lane < 30) *(LAS v4u*)(ub + lane * 16) = (v4u){0, 0, 0, 0};
#pragma unroll
  for (int i = 0; i < 4; ++i) *(LAS v4u*)(tile + (32 * w + 8 * i + (lane >> 3)) * 144 + (lane & 7) * 16) = ut[i];
  BARRIER();
  {
    const unsigned ra = (8 * g + (i16 >> 2)) * 144 + 32 * w + (i16 & 3) * 8;
    const unsigned so = WS_UT + (unsigned)((grp * 16 + 4 * w + (i16 >> 2)) * 16 + r16) * 1024 + g * 64 + (i16 & 3) * 16;
#pragma unroll
    for (int it = 0; it < 4; ++it) {
      const v2u lo = ldstr(tile, ra + it * 32 * 144), hi = ldstr(tile, ra + (it * 32 + 4) * 144);
      __builtin_amdgcn_raw_buffer_store_b128((v4u){lo.x, lo.y, hi.x, hi.y}, rs, so + it * 256, 0, 16);
    }
    asm volatile("s_waitcnt vmcnt(0)" ::: "memory");
    if (lane == 0) __builtin_amdgcn_raw_buffer_store_b64((v2u){tagA, tagB}, rs, WS_FLAG_U + (unsigned)((grp * 4 + w) * 16 + r16) * 8, 0, 16);
  }
  FENCE();
  ST(1);

  f4 kt[3];
  {
    const float dt = rbf(fexp(bf2f(rdt)));
    const unsigned ea = pk2(fexp(bf2f(rA & 0xffffu)), fexp(bfhi(rA)));
    float ar[2] = {-bf2f(ea & 0xffffu) * dt, -bfhi(ea) * dt};
    float ai[2] = {bf2f(rI & 0xffffu) * dt, bfhi(rI) * dt};
    f2 G[2] = {(f2){2.f * (bf2f(rCr & 0xffffu) * dt), 2.f * (bf2f(rCi & 0xffffu) * dt)}, (f2){2.f * (bfhi(rCr) * dt), 2.f * (bfhi(rCi) * dt)}};
    f2 z1[2], z2[2], z4[2], z8[2], z128[2];
#pragma unroll
    for (int m = 0; m < 2; ++m) {
      z1[m] = cexp(ar[m], ai[m]); z8[m] = cexp(8.f * ar[m], 8.f * ai[m]); z128[m] = cexp(128.f * ar[m], 128.f * ai[m]);
      z2[m] = cmul(z1[m], z1[m]); z4[m] = cmul(z2[m], z2[m]);
    }
    auto put = [&](int row, f2 a, f2 b2) { *(LAS v2u*)(kb + row * 528 + lane * 8) = (v2u){pk2(a.x, a.y), pk2(b2.x, b2.y)}; };
    const unsigned fr = i16 * 528 + g * 16;
    v4u rf[8];
    {
      f2 r0 = {1.f, 0.f}, r1 = {1.f, 0.f};
      const f2 c0 = {z8[0].x, -z8[0].y}, c1 = {z8[1].x, -z8[1].y};
#pragma unroll
      for (int j = 0; j < 16; ++j) { put(j, r0, r1); if (j < 15) { r0 = cmul(r0, c0); r1 = cmul(r1, c1); } }
      asm volatile("" ::: "memory");
#pragma unroll
      for (int k = 0; k < 8; ++k) rf[k] = lds16(kb, fr + k * 64);
      asm volatile("" ::: "memory");
    }
    auto tile_mma = [&](int t) {
      asm volatile("" ::: "memory");
      v4u lf[8];
#pragma unroll
      for (int k = 0; k < 8; ++k) lf[k] = lds16(kb, fr + k * 64);
      asm volatile("" ::: "memory");
      kt[t] = (f4){0, 0, 0, 0};
#pragma unroll
      for (int k = 0; k < 8; ++k) kt[t] = mfma16(lf[k], rf[k], kt[t]);
    };
#pragma unroll
    for (int a = 0; a < 2; ++a) {
      f2 v0 = G[0], v1 = G[1];
#pragma unroll
      for (int bb = 0; bb < 8; ++bb) { put(8 * a + bb, v0, v1); if (bb < 7) { v0 = cmul(v0, z1[0]); v1 = cmul(v1, z1[1]); } }
      G[0] = cmul(G[0], z128[0]); G[1] = cmul(G[1], z128[1]);
    }
    tile_mma(0);
#pragma unroll
    for (int a = 2; a < 4; ++a) {
      f2 v0 = G[0], v1 = G[1];
#pragma unroll
      for (int bb = 0; bb < 4; ++bb) { put(4 * (a - 2) + bb, v0, v1); if (bb < 3) { v0 = cmul(v0, z2[0]); v1 = cmul(v1, z2[1]); } }
      G[0] = cmul(G[0], z128[0]); G[1] = cmul(G[1], z128[1]);
    }
#pragma unroll
    for (int a = 4; a < 8; ++a) {
      put(8 + 2 * (a - 4), G[0], G[1]);
      put(9 + 2 * (a - 4), cmul(G[0], z4[0]), cmul(G[1], z4[1]));
      G[0] = cmul(G[0], z128[0]); G[1] = cmul(G[1], z128[1]);
    }
    tile_mma(1);
#pragma unroll
    for (int a = 8; a < 17; ++a) {
      put(a - 8, G[0], G[1]);
      if (a < 16) { G[0] = cmul(G[0], z128[0]); G[1] = cmul(G[1], z128[1]); }
    }
    tile_mma(2);
  }
  FENCE();
  {
    LAS u16* k0 = (LAS u16*)kb;
    {
      unsigned kv[4], pv[4];
#pragma unroll
      for (int r = 0; r < 4; ++r) { kv[r] = pk2(kt[2][r], kt[2][r]); pv[r] = __builtin_amdgcn_update_dpp(0u, kv[r], 0x111, 0xf, 0xf, true); }
      if (g < 2) {
#pragma unroll
        for (int r = 0; r < 4; ++r) {
          const int p0 = 128 * (8 + 4 * g + r) + 8 * i16;
          const unsigned last = (i16 & 1) ? ((kv[r] & 0xffffu) | (pv[r] & 0xffff0000u)) : kv[r];
          *(LAS v2u*)(k0 + 2044 - p0) = (v2u){kv[r], kv[r]};
          *(LAS v2u*)(k0 + 2048 - p0) = (v2u){kv[r], last};
        }
      }
      if (lane == 32) *(LAS v2u*)(k0) = (v2u){kv[0], kv[0]};
    }
    asm volatile("" ::: "memory");
    if (g >= 2) {
#pragma unroll
      for (int q = 0; q < 2; ++q) {
        const int p0 = 128 * (4 + 2 * (g - 2) + q) + 8 * i16;
        const float a0 = kt[1][2 * q], a1 = kt[1][2 * q + 1];
        *(LAS unsigned*)(k0 + 2042 - p0) = pk2(a1, a1);
        *(LAS v2u*)(k0 + 2044 - p0) = (v2u){pk2(a1, a0), pk2(a0, a0)};
        *(LAS unsigned*)(k0 + 2048 - p0) = pk2(a0, a0);
      }
    }
    asm volatile("" ::: "memory");
    if (g < 2) {
      const int p0 = 128 * (2 + g) + 8 * i16;
      const unsigned k3 = pk2(kt[1][3], kt[1][0]);
      k0[2041 - p0] = (u16)k3;
      *(LAS unsigned*)(k0 + 2042 - p0) = pk2(kt[1][2], kt[1][2]);
      *(LAS v2u*)(k0 + 2044 - p0) = (v2u){pk2(kt[1][2], kt[1][1]), pk2(kt[1][0], kt[1][0])};
      k0[2048 - p0] = (u16)(k3 >> 16);
    }
    asm volatile("" ::: "memory");
    {
      const int l0 = 128 * (g >> 1) + 8 * i16 + 4 * (g & 1);
      *(LAS v2u*)(k0 + 2044 - l0) = (v2u){pk2(kt[0][3], kt[0][2]), pk2(kt[0][1], kt[0][0])};
    }
    if (lane < 3) *(LAS v4u*)(k0 + 2048 + 8 * lane) = (v4u){0, 0, 0, 0};
    asm volatile("" ::: "memory");
#pragma unroll
    for (int it = 0; it < 5; ++it) {
      const int e = it * 64 + lane;
      if (e < 258) {
        const v4u a = lds16(kb, e * 16);
        const unsigned nx = *(LAS unsigned*)(kb + e * 16 + 16);
        *(LAS v4u*)(kb + 4160 + e * 16) = (v4u){__builtin_amdgcn_alignbyte(a.y, a.x, 2), __builtin_amdgcn_alignbyte(a.z, a.y, 2),
                                                __builtin_amdgcn_alignbyte(a.w, a.z, 2), __builtin_amdgcn_alignbyte(nx, a.w, 2)};
      }
    }
  }
  FENCE();
  ST(2);
  {
    const unsigned fo = WS_FLAG_U + (unsigned)((grp * 4 + (r16 >> 2)) * 16 + i16) * 8;
#pragma unroll 1
    for (unsigned spin = 0; spin < SPIN_CAP; ++spin) {
      asm volatile("" ::: "memory");
      const v2u f = __builtin_amdgcn_raw_buffer_load_b64(rs, fo, 0, 16);
      if (__all(f.x == tagA && f.y == tagB)) break;
      __builtin_amdgcn_s_sleep(2);
    }
    FENCE();
#pragma unroll
    for (int i = 0; i < 4; ++i) ut[i] = __builtin_amdgcn_raw_buffer_load_b128(rs, WS_UT + (unsigned)((grp * 16 + r16) * 16 + 4 * w + i) * 1024 + lane * 16, 0, 16);
#pragma unroll
    for (int i = 0; i < 4; ++i) *(LAS v4u*)(sm + (lane & 3) * WB + 8448 + 480 + (4 * w + i) * 256 + (lane >> 2) * 16) = ut[i];
  }
  BARRIER();
  ST(3);
  f4 acc[8];
#pragma unroll
  for (int t = 0; t < 8; ++t) acc[t] = (f4){0, 0, 0, 0};
  {
    const int ab = (i16 & 1) ? (2031 - i16 + 8 * g) * 2 : 4160 + (2030 - i16 + 8 * g) * 2;
    const int bb = 448 + 32 * i16 + 16 * g;
#pragma unroll 1
    for (int r = 0; r < 8; ++r) {
      v4u A[8], B[8];
#pragma unroll
      for (int q = 0; q < 8; ++q) {
        LAS unsigned* pa = (LAS unsigned*)(kb + ab - 64 * r - 512 * q);
        A[q] = (v4u){pa[0], pa[1], pa[2], pa[3]};
      }
#pragma unroll
      for (int e = 0; e < 8; ++e) B[e] = lds16(ub, bb - 64 * r + 512 * e);
#pragma unroll
      for (int q = 0; q < 8; ++q)
#pragma unroll
        for (int e = 0; e + q < 8; ++e) acc[e + q] = mfma16(A[q], B[e], acc[e + q]);
    }
  }
  FENCE();
  ST(4);
  {
    const float dk = bf2f(rdk);
    const unsigned so = WS_YT + (unsigned)(((grp * 16 + (i16 >> 3)) * 16 + r16) * 4 + w) * 256 + g * 64 + (i16 & 7) * 8;
#pragma unroll
    for (int t = 0; t < 8; ++t) {
      const v2u uv = *(LAS v2u*)(ub + 480 + (256 * t + 16 * i16 + 4 * g) * 2);
      const unsigned y01 = pk2(acc[t][0], acc[t][1]), y23 = pk2(acc[t][2], acc[t][3]);
      const unsigned s01 = pk2(bf2f(uv.x & 0xffffu) * dk, bfhi(uv.x) * dk), s23 = pk2(bf2f(uv.y & 0xffffu) * dk, bfhi(uv.y) * dk);
      const v2u o = {pk2(bf2f(y01 & 0xffffu) + bf2f(s01 & 0xffffu), bfhi(y01) + bfhi(s01)),
                     pk2(bf2f(y23 & 0xffffu) + bf2f(s23 & 0xffffu), bfhi(y23) + bfhi(s23))};
      __builtin_amdgcn_raw_buffer_store_b64(o, rs, so + t * (2 * 16 * 1024), 0, 16);
    }
    asm volatile("s_waitcnt vmcnt(0)" ::: "memory");
    if (lane == 0) __builtin_amdgcn_raw_buffer_store_b64((v2u){tagA, tagB}, rs, WS_FLAG_Y + (unsigned)(grp * 64 + 4 * r16 + w) * 8, 0, 16);
  }
  FENCE();
  ST(5);
  {
    const unsigned fo = WS_FLAG_Y + (unsigned)(grp * 64 + 16 * w + i16) * 8;
#pragma unroll 1
    for (unsigned spin = 0; spin < SPIN_CAP; ++spin) {
      asm volatile("" ::: "memory");
      const v2u f = __builtin_amdgcn_raw_buffer_load_b64(rs, fo, 0, 16);
      if (__all(f.x == tagA && f.y == tagB)) break;
      __builtin_amdgcn_s_sleep(2);
    }
    FENCE();
#pragma unroll
    for (int i = 0; i < 4; ++i) ut[i] = __builtin_amdgcn_raw_buffer_load_b128(rs, WS_YT + (unsigned)((grp * 16 + r16) * 16 + 4 * w + i) * 1024 + lane * 16, 0, 16);
    LAS u16* tp = (LAS u16*)(tile + (32 * (i16 & 3) + 4 * (i16 >> 2)) * 144) + 16 * w + g;
#pragma unroll
    for (int i = 0; i < 4; ++i) {
#pragma unroll
      for (int k = 0; k < 4; ++k) {
        const int row = 16 * (k >> 1) + 2 * (k & 1);
        tp[row * 72 + 4 * i] = (u16)ut[i][k];
        tp[(row + 1) * 72 + 4 * i] = (u16)(ut[i][k] >> 16);
      }
    }
  }
  BARRIER();
  if (r16 == 0 && tid == 0)
    __hip_atomic_store((GAS unsigned*)(p.ws + WS_EPOCH_B + grp * 256), tagA, __ATOMIC_RELAXED, __HIP_MEMORY_SCOPE_AGENT);
#pragma unroll
  for (int i = 0; i < 8; ++i) {
    const int row = 32 * w + 4 * i + g;
    const v2u v = *(LAS v2u*)(tile + row * 144 + i16 * 8);
    *(GAS v4u*)(p.out + (size_t)(128 * r16 + row) * 1152 + 576 + 64 * grp + 4 * i16) = (v4u){v.x << 16, v.x & 0xffff0000u, v.y << 16, v.y & 0xffff0000u};
  }
  ST(6);
}

__global__ void __launch_bounds__(256, 2) ssm_fused(Params p) {
  extern __shared__ __attribute__((aligned(16))) char smem[];
  LAS char* sm = (LAS char*)smem;
  const int bid = blockIdx.x;
  if (bid < NA) role_a(p, sm, bid);
  else role_b(p, sm, bid - NA);
}

extern "C" void kernel_launch(void* const* d_in, const int* in_sizes, int n_in,
                              void* d_out, int out_size, void* d_ws, size_t ws_size,
                              hipStream_t stream) {
  Params p;
  p.x = (const u16*)d_in[0]; p.Lre = (const u16*)d_in[1]; p.Lim = (const u16*)d_in[2];
  p.Bre = (const u16*)d_in[3]; p.Bim = (const u16*)d_in[4]; p.Cre = (const u16*)d_in[5]; p.Cim = (const u16*)d_in[6];
  p.ldtA = (const u16*)d_in[7]; p.DskA = (const u16*)d_in[8];
  p.u = (const u16*)d_in[9]; p.lA = (const u16*)d_in[10]; p.Aim = (const u16*)d_in[11];
  p.Cr = (const u16*)d_in[12]; p.Ci = (const u16*)d_in[13]; p.ldtB = (const u16*)d_in[14]; p.DskB = (const u16*)d_in[15];
  p.out = (float*)d_out; p.ws = (char*)d_ws;
  (void)hipFuncSetAttribute((const void*)ssm_fused, hipFuncAttributeMaxDynamicSharedMemorySize, LDS_BYTES);
  ssm_fused<<<NA + NB, 256, LDS_BYTES, stream>>>(p);
}
```

```cpp
#include <hip/hip_runtime.h>
#include <hip/hip_bf16.h>
#include <stdint.h>

typedef unsigned short u16;
typedef unsigned v4u __attribute__((ext_vector_type(4)));
typedef unsigned v2u __attribute__((ext_vector_type(2)));
typedef float f2 __attribute__((ext_vector_type(2)));
typedef float f4 __attribute__((ext_vector_type(4)));
typedef __bf16 bf2 __attribute__((ext_vector_type(2)));
typedef __bf16 bf8 __attribute__((ext_vector_type(8)));
typedef short s4 __attribute__((ext_vector_type(4)));
#define GAS __attribute__((address_space(1)))
#define LAS __attribute__((address_space(3)))
#define DI __device__ __forceinline__

constexpr int L = 2048, D = 576, NM = 128;
constexpr int TC = 80, NCH = 26, NS = 9;
constexpr int NA = NCH * NS;
constexpr int NB = 144;
constexpr int LDS_BYTES = 81920;

constexpr unsigned WS_EPOCH_A = 0;
constexpr unsigned WS_EPOCH_B = 256;
constexpr unsigned WS_FLAG_U = 4096;
constexpr unsigned WS_FLAG_Y = 4096 + 8192;
constexpr unsigned WS_E = 32768;
constexpr unsigned WS_E_BYTES = NCH * 36 * 256;
constexpr unsigned WS_H = WS_E + 262144;
constexpr unsigned WS_H_BYTES = NCH * 5 * 36 * 1024;
constexpr unsigned WS_UT = WS_H + WS_H_BYTES;
constexpr unsigned WS_UT_BYTES = 9 * 16 * 16 * 1024;
constexpr unsigned WS_YT = WS_UT + WS_UT_BYTES;
constexpr unsigned WS_YT_BYTES = 9 * 16 * 16 * 1024;
constexpr unsigned WS_TOTAL = WS_YT + WS_YT_BYTES;
#define STC(i) do {} while (0)
#define ST(i) do {} while (0)

#define NPOISON 5
#define GRID_B NB
constexpr unsigned CODE_BYTES = 39000;
constexpr unsigned SPIN_CAP = 1u << 17;

struct Params {
  const u16 *x, *Lre, *Lim, *Bre, *Bim, *Cre, *Cim, *ldtA, *DskA;
  const u16 *u, *lA, *Aim, *Cr, *Ci, *ldtB, *DskB;
  float* out;
  char* ws;
};

DI float bf2f(unsigned v) { return __uint_as_float(v << 16); }
DI float bfhi(unsigned v) { return __uint_as_float(v & 0xffff0000u); }
DI unsigned pk2(float a, float b) { f2 z = {a, b}; return __builtin_bit_cast(unsigned, __builtin_convertvector(z, bf2)); }
DI float rbf(float f) { return bf2f(pk2(f, 0.f) & 0xffffu); }
DI f2 cmul(f2 a, f2 b) {
  f2 t = a * (f2){b.x, b.x};
  return __builtin_elementwise_fma((f2){a.y, a.x}, (f2){-b.y, b.y}, t);
}
DI f2 cfma(f2 a, f2 b, f2 c) {
  f2 t = __builtin_elementwise_fma(a, (f2){b.x, b.x}, c);
  return __builtin_elementwise_fma((f2){a.y, a.x}, (f2){-b.y, b.y}, t);
}
DI float fexp(float x) {
  const float c_hi = 1.44269502162933349609375f, c_lo = 1.925963033500011e-8f;
  float t = x * c_hi;
  float e = __builtin_fmaf(x, c_hi, -t);
  e = __builtin_fmaf(x, c_lo, e);
  float r = __builtin_amdgcn_exp2f(t);
  return __builtin_fmaf(r * e, 0.693147180559945f, r);
}
DI f2 cexp(float re, float im) {
  const float r_hi = 0.15915494f, r_lo = 6.4206382e-9f;
  float m = fexp(re);
  float t = im * r_hi;
  float e = __builtin_fmaf(im, r_hi, -t);
  e = __builtin_fmaf(im, r_lo, e);
  float f = __builtin_amdgcn_fractf(t) + e;
  return (f2){m * __builtin_amdgcn_cosf(f), m * __builtin_amdgcn_sinf(f)};
}
DI f4 mfma16(v4u a, v4u b, f4 c) {
  return __builtin_amdgcn_mfma_f32_16x16x32_bf16(__builtin_bit_cast(bf8, a), __builtin_bit_cast(bf8, b), c, 0, 0, 0);
}
DI v4u ldg16(const void* base, unsigned off, int cst = 0) { return *(const GAS v4u*)((const GAS char*)base + off + cst); }
DI v4u lds16(LAS char* s, unsigned off) { return *(LAS v4u*)(s + off); }
DI v2u ldstr(LAS char* s, unsigned off) {
  return __builtin_bit_cast(v2u, __builtin_amdgcn_ds_read_tr16_b64_v4i16((LAS s4*)(s + off)));
}
#define FENCE() do { __builtin_amdgcn_sched_barrier(0); asm volatile("" ::: "memory"); } while (0)
#define BARRIER() do { __builtin_amdgcn_sched_barrier(0); asm volatile("s_waitcnt lgkmcnt(0)\n\ts_barrier" ::: "memory"); __builtin_amdgcn_sched_barrier(0); } while (0)

struct QMask { unsigned long long e1, o1, e2, o2; };
#define TRC(d, s0, s1, P) "v_cndmask_b32_dpp " d ", " s0 ", " s1 ", vcc quad_perm:" P " row_mask:0xf bank_mask:0xf\n\t"
DI void tr4half(v2u& a0, v2u& a1, v2u& a2, v2u& a3, const QMask& m) {
  unsigned a00 = a0.x, a01 = a0.y, a10 = a1.x, a11 = a1.y, a20 = a2.x, a21 = a2.y, a30 = a3.x, a31 = a3.y;
  unsigned t00, t01, t20, t21;
  asm volatile(
      "s_mov_b64 vcc, %12\n\ts_nop 0\n\t"
      TRC("%8", "%2", "%0", "[1,0,3,2]") TRC("%9", "%3", "%1", "[1,0,3,2]")
      TRC("%10", "%6", "%4", "[1,0,3,2]") TRC("%11", "%7", "%5", "[1,0,3,2]")
      "s_mov_b64 vcc, %13\n\t"
      TRC("%2", "%0", "%2", "[1,0,3,2]") TRC("%3", "%1", "%3", "[1,0,3,2]")
      TRC("%6", "%4", "%6", "[1,0,3,2]") TRC("%7", "%5", "%7", "[1,0,3,2]")
      "s_mov_b64 vcc, %14\n\t"
      TRC("%0", "%10", "%8", "[2,3,0,1]") TRC("%1", "%11", "%9", "[2,3,0,1]")
      TRC("%4", "%6", "%2", "[2,3,0,1]") TRC("%5", "%7", "%3", "[2,3,0,1]")
      "s_mov_b64 vcc, %15\n\t"
      TRC("%10", "%8", "%10", "[2,3,0,1]") TRC("%11", "%9", "%11", "[2,3,0,1]")
      TRC("%6", "%2", "%6", "[2,3,0,1]") TRC("%7", "%3", "%7", "[2,3,0,1]")
      "s_nop 0"
      : "+v"(a00), "+v"(a01), "+v"(a10), "+v"(a11), "+v"(a20), "+v"(a21), "+v"(a30), "+v"(a31),
        "=&v"(t00), "=&v"(t01), "=&v"(t20), "=&v"(t21)
      : "s"(m.e1), "s"(m.o1), "s"(m.e2), "s"(m.o2)
      : "vcc");
  a0 = (v2u){a00, a01}; a1 = (v2u){a20, a21}; a2 = (v2u){t20, t21}; a3 = (v2u){a30, a31};
}
DI void tr4(v4u& a0, v4u& a1, v4u& a2, v4u& a3, const QMask& m) {
  v2u l0 = a0.xy, l1 = a1.xy, l2 = a2.xy, l3 = a3.xy, h0 = a0.zw, h1 = a1.zw, h2 = a2.zw, h3 = a3.zw;
  tr4half(l0, l1, l2, l3, m);
  tr4half(h0, h1, h2, h3, m);
  a0 = (v4u){l0.x, l0.y, h0.x, h0.y}; a1 = (v4u){l1.x, l1.y, h1.x, h1.y};
  a2 = (v4u){l2.x, l2.y, h2.x, h2.y}; a3 = (v4u){l3.x, l3.y, h3.x, h3.y};
}
DI float xsum_groups(float v) {
  auto a = __builtin_amdgcn_permlane16_swap(__float_as_uint(v), __float_as_uint(v), false, false);
  v = __uint_as_float(a[0]) + __uint_as_float(a[1]);
  auto b = __builtin_amdgcn_permlane32_swap(__float_as_uint(v), __float_as_uint(v), false, false);
  return __uint_as_float(b[0]) + __uint_as_float(b[1]);
}
DI unsigned tag2(unsigned t) { return (t * 0x9E3779B1u) ^ 0x5bd1e995u; }
DI unsigned ld_epoch(const char* ws, unsigned off) {
  return __hip_atomic_load((const GAS unsigned*)(ws + off), __ATOMIC_RELAXED, __HIP_MEMORY_SCOPE_AGENT);
}

DI void role_a(const Params& p, LAS char* sm, int bid) {
  const int tid = threadIdx.x, lane = tid & 63;
  const int w = __builtin_amdgcn_readfirstlane(tid >> 6);
  int id; { const int q = NA / 8, r = NA % 8, xcd = bid & 7, off = bid >> 3; id = (xcd < r ? xcd * (q + 1) : r * (q + 1) + (xcd - r) * q) + off; }
  const int c = id / NS, s = id % NS;
  const int i16 = lane & 15, g = lane >> 4, c0 = lane & 3, c1 = (lane >> 2) & 3;
  const QMask qm = {0x5555555555555555ull, 0xAAAAAAAAAAAAAAAAull, 0x3333333333333333ull, 0xCCCCCCCCCCCCCCCCull};
  const auto rs = __builtin_amdgcn_make_buffer_rsrc((void*)p.ws, 0, WS_TOTAL, 0x00020000);
  __builtin_amdgcn_s_setprio(2);
  ST(0); STC(14);

  const unsigned ep = ld_epoch(p.ws, WS_EPOCH_A);
  const int pl = 64 * s + 8 * w + (i16 & 7) + 32 * (i16 >> 3);
  const unsigned rLre = *(const GAS u16*)(p.Lre + pl), rLim = *(const GAS u16*)(p.Lim + pl), rLdt = *(const GAS u16*)(p.ldtA + pl);
  const int kap = 4 * s + w;
  const unsigned hslot = kap < 32 ? 9 * (kap >> 3) + (kap & 7) : 9 * (kap - 32) + 8;
  const unsigned hst = WS_H + (unsigned)(c * 180 + hslot) * 1024 + lane * 16;
  {
    const v4u pz = {~0u, ~0u, ~0u, ~0u};
#pragma unroll
    for (int t = 0; t < NPOISON; ++t) __builtin_amdgcn_raw_buffer_store_b128(pz, rs, hst, t * 36864, 16);
  }
  FENCE();
  auto kb_of = [&](int ord) { int k = s + 1 + ord; return k >= 9 ? k - 9 : k; };
  const auto rx = __builtin_amdgcn_make_buffer_rsrc((void*)p.x, 0, L * D * 2, 0x00020000);
  const auto rbr = __builtin_amdgcn_make_buffer_rsrc((void*)p.Bre, 0, D * D * 2, 0x00020000);
  const auto rbi = __builtin_amdgcn_make_buffer_rsrc((void*)p.Bim, 0, D * D * 2, 0x00020000);
  const unsigned xv0 = (unsigned)(20 * (lane >> 5) + ((lane >> 3) & 3)) * 1152u + (lane & 7) * 16u;
  auto ldx = [&](int ord, int sub) {
    return __builtin_amdgcn_raw_buffer_load_b128(rx, xv0, (c * TC + 40 * (sub & 1) + 4 * (sub >> 1)) * 1152 + kb_of(ord) * 128, 0);
  };
  const unsigned xw = (lane >> 3) * 128 + (((lane & 7) ^ (lane >> 4)) * 16);
  LAS char* xwb = sm + xw + (w >> 1) * 10240 + (w & 1) * 5120;
  const unsigned brow = (unsigned)(4 * (c1 & 1) + 32 * (c1 >> 1)) * 1152u;
  const int bso = (64 * s + 8 * w) * 1152;

  v4u xs[5][5], br[4][4], bi[4][4], bm[4];
  auto load_x = [&](int G) {
#pragma unroll
    for (int i = 0; i < 5; ++i) xs[G][i] = ldx(2 * G + (w >> 1), 5 * (w & 1) + i);
  };
  auto load_b = [&](int G) {
    const unsigned o = brow + (unsigned)(g >> 1 ? kb_of(2 * G + 1) : kb_of(2 * G)) * 128u + (4 * (g & 1) + c0) * 16u;
#pragma unroll
    for (int a = 0; a < 4; ++a) br[G][a] = __builtin_amdgcn_raw_buffer_load_b128(rbr, o + a * 1152, bso, 0);
#pragma unroll
    for (int a = 0; a < 4; ++a) bi[G][a] = __builtin_amdgcn_raw_buffer_load_b128(rbi, o + a * 1152, bso, 0);
  };
  auto store_x = [&](int G) {
#pragma unroll
    for (int i = 0; i < 5; ++i) *(LAS v4u*)(xwb + G * 20480 + i * 1024) = xs[G][i];
  };
  load_x(0); load_b(0); load_x(1); load_b(1); load_x(2); load_b(2);
  FENCE();
  ST(10);

  const float dt = rbf(fexp(bf2f(rLdt)));
  const float lre = bf2f(rLre), lim = bf2f(rLim);
  const float ar = dt * lre, ai = dt * lim;
  const f2 lam = cexp(ar, ai);
  f2 coef2;
  { const float nr = lam.x - 1.0f, ni = lam.y, den = 2.0f / (lre * lre + lim * lim);
    coef2 = (f2){(nr * lre + ni * lim) * den, (ni * lre - nr * lim) * den}; }
  const float fe = (float)(20 * (3 - g));
  const f2 wend = cexp(ar * fe, ai * fe);
  const unsigned tagA = ep + 1u, tagB = tag2(ep + 1u);
  FENCE();
  ST(11);

  f4 aR[5], aI[5];
#pragma unroll
  for (int t = 0; t < 5; ++t) { aR[t] = (f4){0, 0, 0, 0}; aI[t] = (f4){0, 0, 0, 0}; }
  const unsigned fsw = (i16 >> 1) & 3;
  const unsigned ard = (g >> 1) * 10240 + i16 * 128 + (g & 1) * 64;
  auto mma_group = [&](int G) {
    tr4(br[G][0], br[G][1], br[G][2], br[G][3], qm);
    tr4(bi[G][0], bi[G][1], bi[G][2], bi[G][3], qm);
#pragma unroll
    for (int k = 0; k < 4; ++k) {
      v4u xa[5];
#pragma unroll
      for (int t = 0; t < 5; ++t) xa[t] = lds16(sm, G * 20480 + t * 2048 + ard + ((k ^ fsw) * 16));
#pragma unroll
      for (int t = 0; t < 5; ++t) { aR[t] = mfma16(xa[t], br[G][k], aR[t]); aI[t] = mfma16(xa[t], bi[G][k], aI[t]); }
    }
  };
  v4u cf[2][4][4], c8[4];
  const char* cbase = (const char*)(g >> 1 ? p.Cim : p.Cre) + (unsigned)(64 * s + 4 * c1) * 1152u + (32 * (g & 1) + 8 * c0) * 2u;
  auto load_c = [&](int b, int n) {
#pragma unroll
    for (int a = 0; a < 4; ++a) cf[b][n][a] = ldg16(cbase, (unsigned)(16 * n * 1152 + 2 * w * 128), a * 1152 + b * 128);
  };

  store_x(0);
  BARRIER();
  ST(1);
  mma_group(0);
  FENCE();
  ST(12);
  load_x(3);
  load_b(3);
  FENCE();
  store_x(1); BARRIER();
  mma_group(1);
  FENCE();
#pragma unroll
  for (int i = 0; i < 3; ++i) { int sub = w + 4 * i; sub = sub < 10 ? sub : 9; xs[4][i] = ldx(8, sub); }
  {
    const char* mb = (const char*)(c0 >> 1 ? p.Bim : p.Bre);
    const unsigned o = bso + brow + (unsigned)kb_of(8) * 128u + (4 * (g & 1) + 2 * (g >> 1) + (c0 & 1)) * 16u;
#pragma unroll
    for (int a = 0; a < 4; ++a) bm[a] = ldg16(mb, o, a * 1152);
  }
  load_c(0, 0);
  FENCE();
  store_x(2);
  BARRIER();
  mma_group(2);
  FENCE();
  load_c(0, 1); load_c(0, 2);
  FENCE();
  store_x(3);
#pragma unroll
  for (int i = 0; i < 3; ++i) { int sub = w + 4 * i; sub = sub < 10 ? sub : 9; *(LAS v4u*)(sm + sub * 1024 + xw) = xs[4][i]; }
  BARRIER();
  mma_group(3);
  FENCE();
  load_c(0, 3);
  FENCE();
  {
    tr4(bm[0], bm[1], bm[2], bm[3], qm);
    const unsigned a4 = i16 * 128;
#pragma unroll
    for (int k = 0; k < 2; ++k) {
      v4u xa[5];
#pragma unroll
      for (int t = 0; t < 5; ++t) xa[t] = lds16(sm, t * 2048 + a4 + (((4 * (g & 1) + 2 * (g >> 1) + k) ^ fsw) * 16));
#pragma unroll
      for (int t = 0; t < 5; ++t) { aR[t] = mfma16(xa[t], bm[k], aR[t]); aI[t] = mfma16(xa[t], bm[2 + k], aI[t]); }
    }
  }
  FENCE();
  ST(2);

  f2 h[20];
  h[0] = (f2){aR[0][0], aI[0][0]};
#pragma unroll
  for (int j = 1; j < 20; ++j) h[j] = cfma(h[j - 1], lam, (f2){aR[j >> 2][j & 3], aI[j >> 2][j & 3]});
  {
    const f2 e = cmul(h[19], wend);
    const float er = xsum_groups(e.x), ei = xsum_groups(e.y);
    const v4u gr = {__float_as_uint(er), tagA, __float_as_uint(ei), tagB};
    if (g == 0) __builtin_amdgcn_raw_buffer_store_b128(gr, rs, WS_E + (unsigned)(c * 36 + kap) * 256 + i16 * 16, 0, 16);
  }
  FENCE();
  ST(3);
  f2 pre;
  {
    const f2 l20 = cexp(ar * 20.f, ai * 20.f);
    auto r16 = __builtin_amdgcn_permlane16_swap(__float_as_uint(h[19].x), __float_as_uint(h[19].x), false, false);
    auto i16s = __builtin_amdgcn_permlane16_swap(__float_as_uint(h[19].y), __float_as_uint(h[19].y), false, false);
    auto r02 = __builtin_amdgcn_permlane32_swap(r16[0], r16[0], false, false);
    auto i02 = __builtin_amdgcn_permlane32_swap(i16s[0], i16s[0], false, false);
    auto r13 = __builtin_amdgcn_permlane32_swap(r16[1], r16[1], false, false);
    auto i13 = __builtin_amdgcn_permlane32_swap(i16s[1], i16s[1], false, false);
    const f2 e0 = {__uint_as_float(r02[0]), __uint_as_float(i02[0])}, e2 = {__uint_as_float(r02[1]), __uint_as_float(i02[1])};
    const f2 e1 = {__uint_as_float(r13[0]), __uint_as_float(i13[0])};
    const f2 p2 = cfma(e0, l20, e1), p3 = cfma(p2, l20, e2);
    pre = g == 0 ? (f2){0.f, 0.f} : g == 1 ? e0 : g == 2 ? p2 : p3;
  }
  f2 lt[20];
  lt[0] = cmul(coef2, lam);
#pragma unroll
  for (int j = 1; j < 20; ++j) lt[j] = cmul(lt[j - 1], lam);
#pragma unroll
  for (int j = 0; j < 20; ++j) h[j] = cmul(h[j], coef2);
  const float f0 = (float)(80 * ((c - 1 - g) & 3));
  const f2 wq = cexp(ar * f0, ai * f0), w320 = cexp(ar * 320.f, ai * 320.f);
  const float fg = (float)(20 * g);
  const f2 wg20 = cexp(ar * fg, ai * fg);
  tr4(cf[0][0][0], cf[0][0][1], cf[0][0][2], cf[0][0][3], qm);
  tr4(cf[0][1][0], cf[0][1][1], cf[0][1][2], cf[0][1][3], qm);
  tr4(cf[0][2][0], cf[0][2][1], cf[0][2][2], cf[0][2][3], qm);
  tr4(cf[0][3][0], cf[0][3][1], cf[0][3][2], cf[0][3][3], qm);
  FENCE();
  ST(4);

  f2 H = {0.f, 0.f};
  {
    const int kq = lane < 8 ? 8 * w + lane : 32 + w;
    const unsigned oq = WS_E + (unsigned)(c * 36 + kq) * 256;
    const unsigned oe = WS_E + (unsigned)(g * 36 + kap) * 256 + i16 * 16;
    v4u e[7];
#pragma unroll 1
    for (unsigned spin = 0; spin < SPIN_CAP; ++spin) {
      asm volatile("" ::: "memory");
      const v4u q = __builtin_amdgcn_raw_buffer_load_b128(rs, oq, 0, 16);
#pragma unroll
      for (int m = 0; m < 7; ++m) e[m] = __builtin_amdgcn_raw_buffer_load_b128(rs, g + 4 * m < c ? oe + m * (4 * 36 * 256) : oq, 0, 16);
      bool ok = q.y == tagA && q.w == tagB;
#pragma unroll
      for (int m = 0; m < 7; ++m) ok = ok && e[m].y == tagA && e[m].w == tagB;
      if (__all(ok)) break;
      __builtin_amdgcn_s_sleep(1);
    }
#pragma unroll
    for (int m = 0; m < 7; ++m) {
      const f2 nx = cfma(H, w320, (f2){__uint_as_float(e[m].x), __uint_as_float(e[m].z)});
      H = g + 4 * m < c ? nx : H;
    }
    H = cmul(H, wq);
    H = (f2){xsum_groups(H.x), xsum_groups(H.y)};
  }
  FENCE();
  ST(5);
  load_c(1, 0); load_c(1, 1); load_c(1, 2); load_c(1, 3);
#pragma unroll
  for (int a = 0; a < 4; ++a) c8[a] = ldg16(cbase, (unsigned)(16 * w) * 1152u, a * 1152 + 8 * 128);
  FENCE();
  {
    const f2 car = cfma(H, wg20, pre);
    LAS char* tb = sm + 10240 + w * 5376;
#pragma unroll
    for (int t = 0; t < 5; ++t) {
      f2 v[4];
#pragma unroll
      for (int r = 0; r < 4; ++r) v[r] = cfma(lt[4 * t + r], car, h[4 * t + r]);
      *(LAS v2u*)(tb + i16 * 168 + 40 * g + 8 * t) = (v2u){pk2(v[0].x, v[1].x), pk2(v[2].x, v[3].x)};
      *(LAS v2u*)(tb + (16 + i16) * 168 + 40 * g + 8 * t) = (v2u){pk2(-v[0].y, -v[1].y), pk2(-v[2].y, -v[3].y)};
    }
    asm volatile("" ::: "memory");
    const unsigned ra = (8 * g + (i16 >> 2)) * 168 + (i16 & 3) * 8;
#pragma unroll
    for (int t = 0; t < 5; ++t) {
      const v2u lo = ldstr(tb, ra + 32 * t), hi = ldstr(tb, ra + 4 * 168 + 32 * t);
      __builtin_amdgcn_raw_buffer_store_b128((v4u){lo.x, lo.y, hi.x, hi.y}, rs, hst, t * 36864, 16);
    }
  }
  FENCE();
  ST(6);
  tr4(cf[1][0][0], cf[1][0][1], cf[1][0][2], cf[1][0][3], qm);
  tr4(cf[1][1][0], cf[1][1][1], cf[1][1][2], cf[1][1][3], qm);
  tr4(cf[1][2][0], cf[1][2][1], cf[1][2][2], cf[1][2][3], qm);
  tr4(cf[1][3][0], cf[1][3][1], cf[1][3][2], cf[1][3][3], qm);
  tr4(c8[0], c8[1], c8[2], c8[3], qm);
  LAS char* c8s = sm + 32768;
#pragma unroll
  for (int a = 0; a < 4; ++a) *(LAS v4u*)(c8s + (a * 4 + w) * 1024 + lane * 16) = c8[a];
  const v2u dsk = *(const GAS v2u*)(p.DskA + 64 * s + 4 * i16);
  BARRIER();
  ST(7);

  const unsigned hld = WS_H + (unsigned)(c * 180 + 9 * w) * 1024 + lane * 16;
  v4u hf[2][9];
  auto load_h = [&](int t) {
#pragma unroll
    for (int i = 0; i < 9; ++i) hf[t & 1][i] = __builtin_amdgcn_raw_buffer_load_b128(rs, hld + (i & 3) * 1024, t * 36864 + (i >> 2) * 4096, 16);
  };
  auto bad_h = [&](int t) {
    unsigned m = 0;
#pragma unroll
    for (int i = 0; i < 9; ++i) { const v4u v = hf[t & 1][i]; m = max(max(m, v.x), v.y); m = max(max(m, v.z), v.w); }
    return __any(m == ~0u);
  };
  LAS char* red = sm + 49152;
  const float dk[4] = {bf2f(dsk.x & 0xffffu), bfhi(dsk.x), bf2f(dsk.y & 0xffffu), bfhi(dsk.y)};
  load_h(0); load_h(1);
  FENCE();
#pragma unroll
  for (int t = 0; t < 5; ++t) {
#pragma unroll 1
    for (unsigned spin = 0; spin < SPIN_CAP; ++spin) {
      if (!bad_h(t)) break;
      asm volatile("" ::: "memory");
      load_h(t);
    }
    FENCE();
    if (t == 0) ST(8);
    f4 acc[4];
#pragma unroll
    for (int n = 0; n < 4; ++n) acc[n] = mfma16(lds16(c8s, (w * 4 + n) * 1024 + lane * 16), hf[t & 1][8], (f4){0, 0, 0, 0});
#pragma unroll
    for (int i = 0; i < 8; ++i)
#pragma unroll
      for (int n = 0; n < 4; ++n) acc[n] = mfma16(cf[i >> 2][n][i & 3], hf[t & 1][i], acc[n]);
    FENCE();
    if (t + 2 < 5) load_h(t + 2);
    FENCE();
    LAS char* rb = (t & 1) ? sm + 10240 : red;
#pragma unroll
    for (int n = 0; n < 4; ++n) *(LAS f4*)(rb + w * 4352 + i16 * 272 + n * 64 + g * 16) = acc[n];
    BARRIER();
    {
      const int tr = 4 * w + g, tl = 16 * t + tr;
      f4 y = *(LAS f4*)(rb + tr * 272 + i16 * 16);
#pragma unroll
      for (int q = 1; q < 4; ++q) y += *(LAS f4*)(rb + q * 4352 + tr * 272 + i16 * 16);
      const int gq = tl / 20, rem = tl - 20 * gq, rho = 4 * gq + (rem & 3);
      const v2u xv = *(LAS v2u*)(sm + (rem >> 2) * 2048 + rho * 128 + (((i16 >> 1) ^ ((rho >> 1) & 3)) * 16) + (i16 & 1) * 8);
      const unsigned s01 = pk2(bf2f(xv.x & 0xffffu) * dk[0], bfhi(xv.x) * dk[1]), s23 = pk2(bf2f(xv.y & 0xffffu) * dk[2], bfhi(xv.y) * dk[3]);
      y += (f4){bf2f(s01 & 0xffffu), bfhi(s01), bf2f(s23 & 0xffffu), bfhi(s23)};
      const int row = c * TC + tl;
      if (row < L) *(GAS f4*)(p.out + (size_t)row * 1152 + 64 * s + 4 * i16) = y;
    }
    if (t == 0 && id == NA - NS && tid == 0)
      __hip_atomic_store((GAS unsigned*)(p.ws + WS_EPOCH_A), tagA, __ATOMIC_RELAXED, __HIP_MEMORY_SCOPE_AGENT);
    FENCE();
  }
  ST(9); STC(15);
}

DI void role_b(const Params& p, LAS char* sm, int b) {
  const int tid = threadIdx.x, lane = tid & 63;
  const int w = __builtin_amdgcn_readfirstlane(tid >> 6);
  const int grp = b >> 4, r16 = b & 15, ch = 64 * grp + 4 * r16 + w;
  const int i16 = lane & 15, g = lane >> 4;
  const auto rs = __builtin_amdgcn_make_buffer_rsrc((void*)p.ws, 0, WS_TOTAL, 0x00020000);
  ST(0);
  constexpr unsigned WB = 13312;
  LAS char* kb = sm + w * WB;
  LAS char* ub = kb + 8448;
  LAS char* tile = sm + 4 * WB;

  const unsigned ep = ld_epoch(p.ws, WS_EPOCH_B + grp * 256);
  const unsigned rA = *(const GAS unsigned*)(p.lA + ch * NM + 2 * lane), rI = *(const GAS unsigned*)(p.Aim + ch * NM + 2 * lane);
  const unsigned rCr = *(const GAS unsigned*)(p.Cr + ch * NM + 2 * lane), rCi = *(const GAS unsigned*)(p.Ci + ch * NM + 2 * lane);
  const unsigned rdt = *(const GAS u16*)(p.ldtB + ch), rdk = *(const GAS u16*)(p.DskB + ch);
  v4u ut[4];
#pragma unroll
  for (int i = 0; i < 4; ++i) ut[i] = ldg16(p.u, (unsigned)(128 * r16 + 32 * w + 8 * i + (lane >> 3)) * 1152u + grp * 128u + (lane & 7) * 16u);
  FENCE();
  const unsigned tagA = ep + 1u, tagB = tag2(ep + 1u);
  if (lane < 30) *(LAS v4u*)(ub + lane * 16) = (v4u){0, 0, 0, 0};
#pragma unroll
  for (int i = 0; i < 4; ++i) *(LAS v4u*)(tile + (32 * w + 8 * i + (lane >> 3)) * 144 + (lane & 7) * 16) = ut[i];
  BARRIER();
  {
    const unsigned ra = (8 * g + (i16 >> 2)) * 144 + 32 * w + (i16 & 3) * 8;
    const unsigned so = WS_UT + (unsigned)((grp * 16 + 4 * w + (i16 >> 2)) * 16 + r16) * 1024 + g * 64 + (i16 & 3) * 16;
#pragma unroll
    for (int it = 0; it < 4; ++it) {
      const v2u lo = ldstr(tile, ra + it * 32 * 144), hi = ldstr(tile, ra + (it * 32 + 4) * 144);
      __builtin_amdgcn_raw_buffer_store_b128((v4u){lo.x, lo.y, hi.x, hi.y}, rs, so + it * 256, 0, 16);
    }
    asm volatile("s_waitcnt vmcnt(0)" ::: "memory");
    if (lane == 0) __builtin_amdgcn_raw_buffer_store_b64((v2u){tagA, tagB}, rs, WS_FLAG_U + (unsigned)((grp * 4 + w) * 16 + r16) * 8, 0, 16);
  }
  FENCE();
  ST(1);

  f4 kt[3];
  {
    const float dt = rbf(fexp(bf2f(rdt)));
    const unsigned ea = pk2(fexp(bf2f(rA & 0xffffu)), fexp(bfhi(rA)));
    float ar[2] = {-bf2f(ea & 0xffffu) * dt, -bfhi(ea) * dt};
    float ai[2] = {bf2f(rI & 0xffffu) * dt, bfhi(rI) * dt};
    f2 G[2] = {(f2){2.f * (bf2f(rCr & 0xffffu) * dt), 2.f * (bf2f(rCi & 0xffffu) * dt)}, (f2){2.f * (bfhi(rCr) * dt), 2.f * (bfhi(rCi) * dt)}};
    f2 z1[2], z2[2], z4[2], z8[2], z128[2];
#pragma unroll
    for (int m = 0; m < 2; ++m) {
      z1[m] = cexp(ar[m], ai[m]); z8[m] = cexp(8.f * ar[m], 8.f * ai[m]); z128[m] = cexp(128.f * ar[m], 128.f * ai[m]);
      z2[m] = cmul(z1[m], z1[m]); z4[m] = cmul(z2[m], z2[m]);
    }
    auto put = [&](int row, f2 a, f2 b2) { *(LAS v2u*)(kb + row * 528 + lane * 8) = (v2u){pk2(a.x, a.y), pk2(b2.x, b2.y)}; };
    const unsigned fr = i16 * 528 + g * 16;
    v4u rf[8];
    {
      f2 r0 = {1.f, 0.f}, r1 = {1.f, 0.f};
      const f2 c0 = {z8[0].x, -z8[0].y}, c1 = {z8[1].x, -z8[1].y};
#pragma unroll
      for (int j = 0; j < 16; ++j) { put(j, r0, r1); if (j < 15) { r0 = cmul(r0, c0); r1 = cmul(r1, c1); } }
      asm volatile("" ::: "memory");
#pragma unroll
      for (int k = 0; k < 8; ++k) rf[k] = lds16(kb, fr + k * 64);
      asm volatile("" ::: "memory");
    }
    auto tile_mma = [&](int t) {
      asm volatile("" ::: "memory");
      v4u lf[8];
#pragma unroll
      for (int k = 0; k < 8; ++k) lf[k] = lds16(kb, fr + k * 64);
      asm volatile("" ::: "memory");
      kt[t] = (f4){0, 0, 0, 0};
#pragma unroll
      for (int k = 0; k < 8; ++k) kt[t] = mfma16(lf[k], rf[k], kt[t]);
    };
#pragma unroll
    for (int a = 0; a < 2; ++a) {
      f2 v0 = G[0], v1 = G[1];
#pragma unroll
      for (int bb = 0; bb < 8; ++bb) { put(8 * a + bb, v0, v1); if (bb < 7) { v0 = cmul(v0, z1[0]); v1 = cmul(v1, z1[1]); } }
      G[0] = cmul(G[0], z128[0]); G[1] = cmul(G[1], z128[1]);
    }
    tile_mma(0);
#pragma unroll
    for (int a = 2; a < 4; ++a) {
      f2 v0 = G[0], v1 = G[1];
#pragma unroll
      for (int bb = 0; bb < 4; ++bb) { put(4 * (a - 2) + bb, v0, v1); if (bb < 3) { v0 = cmul(v0, z2[0]); v1 = cmul(v1, z2[1]); } }
      G[0] = cmul(G[0], z128[0]); G[1] = cmul(G[1], z128[1]);
    }
#pragma unroll
    for (int a = 4; a < 8; ++a) {
      put(8 + 2 * (a - 4), G[0], G[1]);
      put(9 + 2 * (a - 4), cmul(G[0], z4[0]), cmul(G[1], z4[1]));
      G[0] = cmul(G[0], z128[0]); G[1] = cmul(G[1], z128[1]);
    }
    tile_mma(1);
#pragma unroll
    for (int a = 8; a < 17; ++a) {
      put(a - 8, G[0], G[1]);
      if (a < 16) { G[0] = cmul(G[0], z128[0]); G[1] = cmul(G[1], z128[1]); }
    }
    tile_mma(2);
  }
  FENCE();
  {
    LAS u16* k0 = (LAS u16*)kb;
    {
      unsigned kv[4], pv[4];
#pragma unroll
      for (int r = 0; r < 4; ++r) { kv[r] = pk2(kt[2][r], kt[2][r]); pv[r] = __builtin_amdgcn_update_dpp(0u, kv[r], 0x111, 0xf, 0xf, true); }
      if (g < 2) {
#pragma unroll
        for (int r = 0; r < 4; ++r) {
          const int p0 = 128 * (8 + 4 * g + r) + 8 * i16;
          const unsigned last = (i16 & 1) ? ((kv[r] & 0xffffu) | (pv[r] & 0xffff0000u)) : kv[r];
          *(LAS v2u*)(k0 + 2044 - p0) = (v2u){kv[r], kv[r]};
          *(LAS v2u*)(k0 + 2048 - p0) = (v2u){kv[r], last};
        }
      }
      if (lane == 32) *(LAS v2u*)(k0) = (v2u){kv[0], kv[0]};
    }
    asm volatile("" ::: "memory");
    if (g >= 2) {
#pragma unroll
      for (int q = 0; q < 2; ++q) {
        const int p0 = 128 * (4 + 2 * (g - 2) + q) + 8 * i16;
        const float a0 = kt[1][2 * q], a1 = kt[1][2 * q + 1];
        *(LAS unsigned*)(k0 + 2042 - p0) = pk2(a1, a1);
        *(LAS v2u*)(k0 + 2044 - p0) = (v2u){pk2(a1, a0), pk2(a0, a0)};
        *(LAS unsigned*)(k0 + 2048 - p0) = pk2(a0, a0);
      }
    }
    asm volatile("" ::: "memory");
    if (g < 2) {
      const int p0 = 128 * (2 + g) + 8 * i16;
      const unsigned k3 = pk2(kt[1][3], kt[1][0]);
      k0[2041 - p0] = (u16)k3;
      *(LAS unsigned*)(k0 + 2042 - p0) = pk2(kt[1][2], kt[1][2]);
      *(LAS v2u*)(k0 + 2044 - p0) = (v2u){pk2(kt[1][2], kt[1][1]), pk2(kt[1][0], kt[1][0])};
      k0[2048 - p0] = (u16)(k3 >> 16);
    }
    asm volatile("" ::: "memory");
    {
      const int l0 = 128 * (g >> 1) + 8 * i16 + 4 * (g & 1);
      *(LAS v2u*)(k0 + 2044 - l0) = (v2u){pk2(kt[0][3], kt[0][2]), pk2(kt[0][1], kt[0][0])};
    }
    if (lane < 3) *(LAS v4u*)(k0 + 2048 + 8 * lane) = (v4u){0, 0, 0, 0};
    asm volatile("" ::: "memory");
#pragma unroll
    for (int it = 0; it < 5; ++it) {
      const int e = it * 64 + lane;
      if (e < 258) {
        const v4u a = lds16(kb, e * 16);
        const unsigned nx = *(LAS unsigned*)(kb + e * 16 + 16);
        *(LAS v4u*)(kb + 4160 + e * 16) = (v4u){__builtin_amdgcn_alignbyte(a.y, a.x, 2), __builtin_amdgcn_alignbyte(a.z, a.y, 2),
                                                __builtin_amdgcn_alignbyte(a.w, a.z, 2), __builtin_amdgcn_alignbyte(nx, a.w, 2)};
      }
    }
  }
  FENCE();
  ST(2);
  {
    const unsigned fo = WS_FLAG_U + (unsigned)((grp * 4 + (r16 >> 2)) * 16 + i16) * 8;
#pragma unroll 1
    for (unsigned spin = 0; spin < SPIN_CAP; ++spin) {
      asm volatile("" ::: "memory");
      const v2u f = __builtin_amdgcn_raw_buffer_load_b64(rs, fo, 0, 16);
      if (__all(f.x == tagA && f.y == tagB)) break;
      __builtin_amdgcn_s_sleep(2);
    }
    FENCE();
#pragma unroll
    for (int i = 0; i < 4; ++i) ut[i] = __builtin_amdgcn_raw_buffer_load_b128(rs, WS_UT + (unsigned)((grp * 16 + r16) * 16 + 4 * w + i) * 1024 + lane * 16, 0, 16);
#pragma unroll
    for (int i = 0; i < 4; ++i) *(LAS v4u*)(sm + (lane & 3) * WB + 8448 + 480 + (4 * w + i) * 256 + (lane >> 2) * 16) = ut[i];
  }
  BARRIER();
  ST(3);
  f4 acc[8];
#pragma unroll
  for (int t = 0; t < 8; ++t) acc[t] = (f4){0, 0, 0, 0};
  {
    const int ab = (i16 & 1) ? (2031 - i16 + 8 * g) * 2 : 4160 + (2030 - i16 + 8 * g) * 2;
    const int bb = 448 + 32 * i16 + 16 * g;
#pragma unroll 1
    for (int r = 0; r < 8; ++r) {
      v4u A[8], B[8];
#pragma unroll
      for (int q = 0; q < 8; ++q) {
        LAS unsigned* pa = (LAS unsigned*)(kb + ab - 64 * r - 512 * q);
        A[q] = (v4u){pa[0], pa[1], pa[2], pa[3]};
      }
#pragma unroll
      for (int e = 0; e < 8; ++e) B[e] = lds16(ub, bb - 64 * r + 512 * e);
#pragma unroll
      for (int q = 0; q < 8; ++q)
#pragma unroll
        for (int e = 0; e + q < 8; ++e) acc[e + q] = mfma16(A[q], B[e], acc[e + q]);
    }
  }
  FENCE();
  ST(4);
  {
    const float dk = bf2f(rdk);
    const unsigned so = WS_YT + (unsigned)(((grp * 16 + (i16 >> 3)) * 16 + r16) * 4 + w) * 256 + g * 64 + (i16 & 7) * 8;
#pragma unroll
    for (int t = 0; t < 8; ++t) {
      const v2u uv = *(LAS v2u*)(ub + 480 + (256 * t + 16 * i16 + 4 * g) * 2);
      const unsigned y01 = pk2(acc[t][0], acc[t][1]), y23 = pk2(acc[t][2], acc[t][3]);
      const unsigned s01 = pk2(bf2f(uv.x & 0xffffu) * dk, bfhi(uv.x) * dk), s23 = pk2(bf2f(uv.y & 0xffffu) * dk, bfhi(uv.y) * dk);
      const v2u o = {pk2(bf2f(y01 & 0xffffu) + bf2f(s01 & 0xffffu), bfhi(y01) + bfhi(s01)),
                     pk2(bf2f(y23 & 0xffffu) + bf2f(s23 & 0xffffu), bfhi(y23) + bfhi(s23))};
      __builtin_amdgcn_raw_buffer_store_b64(o, rs, so + t * (2 * 16 * 1024), 0, 16);
    }
    asm volatile("s_waitcnt vmcnt(0)" ::: "memory");
    if (lane == 0) __builtin_amdgcn_raw_buffer_store_b64((v2u){tagA, tagB}, rs, WS_FLAG_Y + (unsigned)(grp * 64 + 4 * r16 + w) * 8, 0, 16);
  }
  FENCE();
  ST(5);
  {
    const unsigned fo = WS_FLAG_Y + (unsigned)(grp * 64 + 16 * w + i16) * 8;
#pragma unroll 1
    for (unsigned spin = 0; spin < SPIN_CAP; ++spin) {
      asm volatile("" ::: "memory");
      const v2u f = __builtin_amdgcn_raw_buffer_load_b64(rs, fo, 0, 16);
      if (__all(f.x == tagA && f.y == tagB)) break;
      __builtin_amdgcn_s_sleep(2);
    }
    FENCE();
#pragma unroll
    for (int i = 0; i < 4; ++i) ut[i] = __builtin_amdgcn_raw_buffer_load_b128(rs, WS_YT + (unsigned)((grp * 16 + r16) * 16 + 4 * w + i) * 1024 + lane * 16, 0, 16);
    LAS u16* tp = (LAS u16*)(tile + (32 * (i16 & 3) + 4 * (i16 >> 2)) * 144) + 16 * w + g;
#pragma unroll
    for (int i = 0; i < 4; ++i) {
#pragma unroll
      for (int k = 0; k < 4; ++k) {
        const int row = 16 * (k >> 1) + 2 * (k & 1);
        tp[row * 72 + 4 * i] = (u16)ut[i][k];
        tp[(row + 1) * 72 + 4 * i] = (u16)(ut[i][k] >> 16);
      }
    }
  }
  BARRIER();
  if (r16 == 0 && tid == 0)
    __hip_atomic_store((GAS unsigned*)(p.ws + WS_EPOCH_B + grp * 256), tagA, __ATOMIC_RELAXED, __HIP_MEMORY_SCOPE_AGENT);
#pragma unroll
  for (int i = 0; i < 8; ++i) {
    const int row = 32 * w + 4 * i + g;
    const v2u v = *(LAS v2u*)(tile + row * 144 + i16 * 8);
    *(GAS v4u*)(p.out + (size_t)(128 * r16 + row) * 1152 + 576 + 64 * grp + 4 * i16) = (v4u){v.x << 16, v.x & 0xffff0000u, v.y << 16, v.y & 0xffff0000u};
  }
  ST(6);
}

__global__ void __launch_bounds__(256, 2) ssm_fused(Params p) {
  extern __shared__ __attribute__((aligned(16))) char smem[];
  LAS char* sm = (LAS char*)smem;
  const int bid = blockIdx.x;
  unsigned long long pc;
  asm volatile("s_getpc_b64 %0" : "=s"(pc));
  pc &= ~255ull;
  const unsigned part = (((unsigned)bid >> 3) * 4 + (threadIdx.x >> 6)) % 5u;
  unsigned off = part * 8192u + (threadIdx.x & 63) * 128u;
  off = off < CODE_BYTES ? off : 0;
  const unsigned pf = *(const GAS unsigned*)((const GAS char*)pc + off);
  if (bid < NA) role_a(p, sm, bid);
  else role_b(p, sm, bid - NA);
  asm volatile("" :: "v"(pf));
}

extern "C" void kernel_launch(void* const* d_in, const int* in_sizes, int n_in,
                              void* d_out, int out_size, void* d_ws, size_t ws_size,
                              hipStream_t stream) {
  Params p;
  p.x = (const u16*)d_in[0]; p.Lre = (const u16*)d_in[1]; p.Lim = (const u16*)d_in[2];
  p.Bre = (const u16*)d_in[3]; p.Bim = (const u16*)d_in[4]; p.Cre = (const u16*)d_in[5]; p.Cim = (const u16*)d_in[6];
  p.ldtA = (const u16*)d_in[7]; p.DskA = (const u16*)d_in[8];
  p.u = (const u16*)d_in[9]; p.lA = (const u16*)d_in[10]; p.Aim = (const u16*)d_in[11];
  p.Cr = (const u16*)d_in[12]; p.Ci = (const u16*)d_in[13]; p.ldtB = (const u16*)d_in[14]; p.DskB = (const u16*)d_in[15];
  p.out = (float*)d_out; p.ws = (char*)d_ws;
  (void)hipFuncSetAttribute((const void*)ssm_fused, hipFuncAttributeMaxDynamicSharedMemorySize, LDS_BYTES);
  ssm_fused<<<NA + GRID_B, 256, LDS_BYTES, stream>>>(p);
}
```

```cpp
#define PROBE 0
#define PROBE_BID 100
#include <hip/hip_runtime.h>
#include <hip/hip_bf16.h>
#include <stdint.h>

typedef unsigned short u16;
typedef unsigned v4u __attribute__((ext_vector_type(4)));
typedef unsigned v2u __attribute__((ext_vector_type(2)));
typedef float f2 __attribute__((ext_vector_type(2)));
typedef float f4 __attribute__((ext_vector_type(4)));
typedef __bf16 bf2 __attribute__((ext_vector_type(2)));
typedef __bf16 bf8 __attribute__((ext_vector_type(8)));
typedef short s4 __attribute__((ext_vector_type(4)));
#define GAS __attribute__((address_space(1)))
#define LAS __attribute__((address_space(3)))
#define DI __device__ __forceinline__

constexpr int L = 2048, D = 576, NM = 128;
constexpr int TC = 80, NCH = 26, NS = 9;
constexpr int NA = NCH * NS;
constexpr int NB = 144;
constexpr int LDS_BYTES = 81920;

constexpr unsigned WS_EPOCH_A = 0;
constexpr unsigned WS_EPOCH_B = 256;
constexpr unsigned WS_FLAG_U = 4096;
constexpr unsigned WS_FLAG_Y = 4096 + 8192;
constexpr unsigned WS_E = 32768;
constexpr unsigned WS_E_BYTES = NCH * 36 * 256;
constexpr unsigned WS_H = WS_E + 262144;
constexpr unsigned WS_H_BYTES = NCH * 5 * 36 * 1024;
constexpr unsigned WS_UT = WS_H + WS_H_BYTES;
constexpr unsigned WS_UT_BYTES = 9 * 16 * 16 * 1024;
constexpr unsigned WS_YT = WS_UT + WS_UT_BYTES;
constexpr unsigned WS_YT_BYTES = 9 * 16 * 16 * 1024;
constexpr unsigned WS_TOTAL = WS_YT + WS_YT_BYTES;
#define ST(i) do { if ((i) == 0) t0p = __builtin_amdgcn_s_memrealtime(); if ((i) == PROBE) tpp = __builtin_amdgcn_s_memrealtime(); } while (0)
#define STC(i) do { if ((i) == 14) c0p = __builtin_amdgcn_s_memtime(); if ((i) == 15 && PROBE == 15) tpp = t0p + (__builtin_amdgcn_s_memtime() - c0p) / 20; } while (0)

#define NPOISON 5
#define GRID_B NB
constexpr unsigned CODE_BYTES = 39000;
constexpr unsigned SPIN_CAP = 1u << 17;

struct Params {
  const u16 *x, *Lre, *Lim, *Bre, *Bim, *Cre, *Cim, *ldtA, *DskA;
  const u16 *u, *lA, *Aim, *Cr, *Ci, *ldtB, *DskB;
  float* out;
  char* ws;
};

DI float bf2f(unsigned v) { return __uint_as_float(v << 16); }
DI float bfhi(unsigned v) { return __uint_as_float(v & 0xffff0000u); }
DI unsigned pk2(float a, float b) { f2 z = {a, b}; return __builtin_bit_cast(unsigned, __builtin_convertvector(z, bf2)); }
DI float rbf(float f) { return bf2f(pk2(f, 0.f) & 0xffffu); }
DI f2 cmul(f2 a, f2 b) {
  f2 t = a * (f2){b.x, b.x};
  return __builtin_elementwise_fma((f2){a.y, a.x}, (f2){-b.y, b.y}, t);
}
DI f2 cfma(f2 a, f2 b, f2 c) {
  f2 t = __builtin_elementwise_fma(a, (f2){b.x, b.x}, c);
  return __builtin_elementwise_fma((f2){a.y, a.x}, (f2){-b.y, b.y}, t);
}
DI float fexp(float x) {
  const float c_hi = 1.44269502162933349609375f, c_lo = 1.925963033500011e-8f;
  float t = x * c_hi;
  float e = __builtin_fmaf(x, c_hi, -t);
  e = __builtin_fmaf(x, c_lo, e);
  float r = __builtin_amdgcn_exp2f(t);
  return __builtin_fmaf(r * e, 0.693147180559945f, r);
}
DI f2 cexp(float re, float im) {
  const float r_hi = 0.15915494f, r_lo = 6.4206382e-9f;
  float m = fexp(re);
  float t = im * r_hi;
  float e = __builtin_fmaf(im, r_hi, -t);
  e = __builtin_fmaf(im, r_lo, e);
  float f = __builtin_amdgcn_fractf(t) + e;
  return (f2){m * __builtin_amdgcn_cosf(f), m * __builtin_amdgcn_sinf(f)};
}
DI f4 mfma16(v4u a, v4u b, f4 c) {
  return __builtin_amdgcn_mfma_f32_16x16x32_bf16(__builtin_bit_cast(bf8, a), __builtin_bit_cast(bf8, b), c, 0, 0, 0);
}
DI v4u ldg16(const void* base, unsigned off, int cst = 0) { return *(const GAS v4u*)((const GAS char*)base + off + cst); }
DI v4u lds16(LAS char* s, unsigned off) { return *(LAS v4u*)(s + off); }
DI v2u ldstr(LAS char* s, unsigned off) {
  return __builtin_bit_cast(v2u, __builtin_amdgcn_ds_read_tr16_b64_v4i16((LAS s4*)(s + off)));
}
#define FENCE() do { __builtin_amdgcn_sched_barrier(0); asm volatile("" ::: "memory"); } while (0)
#define BARRIER() do { __builtin_amdgcn_sched_barrier(0); asm volatile("s_waitcnt lgkmcnt(0)\n\ts_barrier" ::: "memory"); __builtin_amdgcn_sched_barrier(0); } while (0)

struct QMask { unsigned long long e1, o1, e2, o2; };
#define TRC(d, s0, s1, P) "v_cndmask_b32_dpp " d ", " s0 ", " s1 ", vcc quad_perm:" P " row_mask:0xf bank_mask:0xf\n\t"
DI void tr4half(v2u& a0, v2u& a1, v2u& a2, v2u& a3, const QMask& m) {
  unsigned a00 = a0.x, a01 = a0.y, a10 = a1.x, a11 = a1.y, a20 = a2.x, a21 = a2.y, a30 = a3.x, a31 = a3.y;
  unsigned t00, t01, t20, t21;
  asm volatile(
      "s_mov_b64 vcc, %12\n\ts_nop 0\n\t"
      TRC("%8", "%2", "%0", "[1,0,3,2]") TRC("%9", "%3", "%1", "[1,0,3,2]")
      TRC("%10", "%6", "%4", "[1,0,3,2]") TRC("%11", "%7", "%5", "[1,0,3,2]")
      "s_mov_b64 vcc, %13\n\t"
      TRC("%2", "%0", "%2", "[1,0,3,2]") TRC("%3", "%1", "%3", "[1,0,3,2]")
      TRC("%6", "%4", "%6", "[1,0,3,2]") TRC("%7", "%5", "%7", "[1,0,3,2]")
      "s_mov_b64 vcc, %14\n\t"
      TRC("%0", "%10", "%8", "[2,3,0,1]") TRC("%1", "%11", "%9", "[2,3,0,1]")
      TRC("%4", "%6", "%2", "[2,3,0,1]") TRC("%5", "%7", "%3", "[2,3,0,1]")
      "s_mov_b64 vcc, %15\n\t"
      TRC("%10", "%8", "%10", "[2,3,0,1]") TRC("%11", "%9", "%11", "[2,3,0,1]")
      TRC("%6", "%2", "%6", "[2,3,0,1]") TRC("%7", "%3", "%7", "[2,3,0,1]")
      "s_nop 0"
      : "+v"(a00), "+v"(a01), "+v"(a10), "+v"(a11), "+v"(a20), "+v"(a21), "+v"(a30), "+v"(a31),
        "=&v"(t00), "=&v"(t01), "=&v"(t20), "=&v"(t21)
      : "s"(m.e1), "s"(m.o1), "s"(m.e2), "s"(m.o2)
      : "vcc");
  a0 = (v2u){a00, a01}; a1 = (v2u){a20, a21}; a2 = (v2u){t20, t21}; a3 = (v2u){a30, a31};
}
DI void tr4(v4u& a0, v4u& a1, v4u& a2, v4u& a3, const QMask& m) {
  v2u l0 = a0.xy, l1 = a1.xy, l2 = a2.xy, l3 = a3.xy, h0 = a0.zw, h1 = a1.zw, h2 = a2.zw, h3 = a3.zw;
  tr4half(l0, l1, l2, l3, m);
  tr4half(h0, h1, h2, h3, m);
  a0 = (v4u){l0.x, l0.y, h0.x, h0.y}; a1 = (v4u){l1.x, l1.y, h1.x, h1.y};
  a2 = (v4u){l2.x, l2.y, h2.x, h2.y}; a3 = (v4u){l3.x, l3.y, h3.x, h3.y};
}
DI float xsum_groups(float v) {
  auto a = __builtin_amdgcn_permlane16_swap(__float_as_uint(v), __float_as_uint(v), false, false);
  v = __uint_as_float(a[0]) + __uint_as_float(a[1]);
  auto b = __builtin_amdgcn_permlane32_swap(__float_as_uint(v), __float_as_uint(v), false, false);
  return __uint_as_float(b[0]) + __uint_as_float(b[1]);
}
DI unsigned tag2(unsigned t) { return (t * 0x9E3779B1u) ^ 0x5bd1e995u; }
DI unsigned ld_epoch(const char* ws, unsigned off) {
  return __hip_atomic_load((const GAS unsigned*)(ws + off), __ATOMIC_RELAXED, __HIP_MEMORY_SCOPE_AGENT);
}

DI void role_a(const Params& p, LAS char* sm, int bid) {
  unsigned long long t0p = 0, tpp = 0, c0p = 0;
  const int tid = threadIdx.x, lane = tid & 63;
  const int w = __builtin_amdgcn_readfirstlane(tid >> 6);
  int id; { const int q = NA / 8, r = NA % 8, xcd = bid & 7, off = bid >> 3; id = (xcd < r ? xcd * (q + 1) : r * (q + 1) + (xcd - r) * q) + off; }
  const int c = id / NS, s = id % NS;
  const int i16 = lane & 15, g = lane >> 4, c0 = lane & 3, c1 = (lane >> 2) & 3;
  const QMask qm = {0x5555555555555555ull, 0xAAAAAAAAAAAAAAAAull, 0x3333333333333333ull, 0xCCCCCCCCCCCCCCCCull};
  const auto rs = __builtin_amdgcn_make_buffer_rsrc((void*)p.ws, 0, WS_TOTAL, 0x00020000);
  __builtin_amdgcn_s_setprio(2);
  ST(0); STC(14);

  const unsigned ep = ld_epoch(p.ws, WS_EPOCH_A);
  const int pl = 64 * s + 8 * w + (i16 & 7) + 32 * (i16 >> 3);
  const unsigned rLre = *(const GAS u16*)(p.Lre + pl), rLim = *(const GAS u16*)(p.Lim + pl), rLdt = *(const GAS u16*)(p.ldtA + pl);
  const int kap = 4 * s + w;
  const unsigned hslot = kap < 32 ? 9 * (kap >> 3) + (kap & 7) : 9 * (kap - 32) + 8;
  const unsigned hst = WS_H + (unsigned)(c * 180 + hslot) * 1024 + lane * 16;
  {
    const v4u pz = {~0u, ~0u, ~0u, ~0u};
#pragma unroll
    for (int t = 0; t < NPOISON; ++t) __builtin_amdgcn_raw_buffer_store_b128(pz, rs, hst, t * 36864, 16);
  }
  FENCE();
  auto kb_of = [&](int ord) { int k = s + 1 + ord; return k >= 9 ? k - 9 : k; };
  const auto rx = __builtin_amdgcn_make_buffer_rsrc((void*)p.x, 0, L * D * 2, 0x00020000);
  const auto rbr = __builtin_amdgcn_make_buffer_rsrc((void*)p.Bre, 0, D * D * 2, 0x00020000);
  const auto rbi = __builtin_amdgcn_make_buffer_rsrc((void*)p.Bim, 0, D * D * 2, 0x00020000);
  const unsigned xv0 = (unsigned)(20 * (lane >> 5) + ((lane >> 3) & 3)) * 1152u + (lane & 7) * 16u;
  auto ldx = [&](int ord, int sub) {
    return __builtin_amdgcn_raw_buffer_load_b128(rx, xv0, (c * TC + 40 * (sub & 1) + 4 * (sub >> 1)) * 1152 + kb_of(ord) * 128, 0);
  };
  const unsigned xw = (lane >> 3) * 128 + (((lane & 7) ^ (lane >> 4)) * 16);
  LAS char* xwb = sm + xw + (w >> 1) * 10240 + (w & 1) * 5120;
  const unsigned brow = (unsigned)(4 * (c1 & 1) + 32 * (c1 >> 1)) * 1152u;
  const int bso = (64 * s + 8 * w) * 1152;

  v4u xs[5][5], br[4][4], bi[4][4], bm[4];
  auto load_x = [&](int G) {
#pragma unroll
    for (int i = 0; i < 5; ++i) xs[G][i] = ldx(2 * G + (w >> 1), 5 * (w & 1) + i);
  };
  auto load_b = [&](int G) {
    const unsigned o = brow + (unsigned)(g >> 1 ? kb_of(2 * G + 1) : kb_of(2 * G)) * 128u + (4 * (g & 1) + c0) * 16u;
#pragma unroll
    for (int a = 0; a < 4; ++a) br[G][a] = __builtin_amdgcn_raw_buffer_load_b128(rbr, o + a * 1152, bso, 0);
#pragma unroll
    for (int a = 0; a < 4; ++a) bi[G][a] = __builtin_amdgcn_raw_buffer_load_b128(rbi, o + a * 1152, bso, 0);
  };
  auto store_x = [&](int G) {
#pragma unroll
    for (int i = 0; i < 5; ++i) *(LAS v4u*)(xwb + G * 20480 + i * 1024) = xs[G][i];
  };
  load_x(0); load_b(0); load_x(1); load_b(1); load_x(2); load_b(2);
  FENCE();
  ST(10);

  const float dt = rbf(fexp(bf2f(rLdt)));
  const float lre = bf2f(rLre), lim = bf2f(rLim);
  const float ar = dt * lre, ai = dt * lim;
  const f2 lam = cexp(ar, ai);
  f2 coef2;
  { const float nr = lam.x - 1.0f, ni = lam.y, den = 2.0f / (lre * lre + lim * lim);
    coef2 = (f2){(nr * lre + ni * lim) * den, (ni * lre - nr * lim) * den}; }
  const float fe = (float)(20 * (3 - g));
  const f2 wend = cexp(ar * fe, ai * fe);
  const unsigned tagA = ep + 1u, tagB = tag2(ep + 1u);
  FENCE();
  ST(11);

  f4 aR[5], aI[5];
#pragma unroll
  for (int t = 0; t < 5; ++t) { aR[t] = (f4){0, 0, 0, 0}; aI[t] = (f4){0, 0, 0, 0}; }
  const unsigned fsw = (i16 >> 1) & 3;
  const unsigned ard = (g >> 1) * 10240 + i16 * 128 + (g & 1) * 64;
  auto mma_group = [&](int G) {
    tr4(br[G][0], br[G][1], br[G][2], br[G][3], qm);
    tr4(bi[G][0], bi[G][1], bi[G][2], bi[G][3], qm);
#pragma unroll
    for (int k = 0; k < 4; ++k) {
      v4u xa[5];
#pragma unroll
      for (int t = 0; t < 5; ++t) xa[t] = lds16(sm, G * 20480 + t * 2048 + ard + ((k ^ fsw) * 16));
#pragma unroll
      for (int t = 0; t < 5; ++t) { aR[t] = mfma16(xa[t], br[G][k], aR[t]); aI[t] = mfma16(xa[t], bi[G][k], aI[t]); }
    }
  };
  v4u cf[2][4][4], c8[4];
  const char* cbase = (const char*)(g >> 1 ? p.Cim : p.Cre) + (unsigned)(64 * s + 4 * c1) * 1152u + (32 * (g & 1) + 8 * c0) * 2u;
  auto load_c = [&](int b, int n) {
#pragma unroll
    for (int a = 0; a < 4; ++a) cf[b][n][a] = ldg16(cbase, (unsigned)(16 * n * 1152 + 2 * w * 128), a * 1152 + b * 128);
  };

  store_x(0);
  BARRIER();
  ST(1);
  mma_group(0);
  FENCE();
  ST(12);
  load_x(3);
  load_b(3);
  FENCE();
  store_x(1); BARRIER();
  mma_group(1);
  FENCE();
#pragma unroll
  for (int i = 0; i < 3; ++i) { int sub = w + 4 * i; sub = sub < 10 ? sub : 9; xs[4][i] = ldx(8, sub); }
  {
    const char* mb = (const char*)(c0 >> 1 ? p.Bim : p.Bre);
    const unsigned o = bso + brow + (unsigned)kb_of(8) * 128u + (4 * (g & 1) + 2 * (g >> 1) + (c0 & 1)) * 16u;
#pragma unroll
    for (int a = 0; a < 4; ++a) bm[a] = ldg16(mb, o, a * 1152);
  }
  load_c(0, 0);
  FENCE();
  store_x(2);
  BARRIER();
  mma_group(2);
  FENCE();
  load_c(0, 1); load_c(0, 2);
  FENCE();
  store_x(3);
#pragma unroll
  for (int i = 0; i < 3; ++i) { int sub = w + 4 * i; sub = sub < 10 ? sub : 9; *(LAS v4u*)(sm + sub * 1024 + xw) = xs[4][i]; }
  BARRIER();
  mma_group(3);
  FENCE();
  load_c(0, 3);
  FENCE();
  {
    tr4(bm[0], bm[1], bm[2], bm[3], qm);
    const unsigned a4 = i16 * 128;
#pragma unroll
    for (int k = 0; k < 2; ++k) {
      v4u xa[5];
#pragma unroll
      for (int t = 0; t < 5; ++t) xa[t] = lds16(sm, t * 2048 + a4 + (((4 * (g & 1) + 2 * (g >> 1) + k) ^ fsw) * 16));
#pragma unroll
      for (int t = 0; t < 5; ++t) { aR[t] = mfma16(xa[t], bm[k], aR[t]); aI[t] = mfma16(xa[t], bm[2 + k], aI[t]); }
    }
  }
  FENCE();
  ST(2);

  f2 h[20];
  h[0] = (f2){aR[0][0], aI[0][0]};
#pragma unroll
  for (int j = 1; j < 20; ++j) h[j] = cfma(h[j - 1], lam, (f2){aR[j >> 2][j & 3], aI[j >> 2][j & 3]});
  {
    const f2 e = cmul(h[19], wend);
    const float er = xsum_groups(e.x), ei = xsum_groups(e.y);
    const v4u gr = {__float_as_uint(er), tagA, __float_as_uint(ei), tagB};
    if (g == 0) __builtin_amdgcn_raw_buffer_store_b128(gr, rs, WS_E + (unsigned)(c * 36 + kap) * 256 + i16 * 16, 0, 16);
  }
  FENCE();
  ST(3);
  f2 pre;
  {
    const f2 l20 = cexp(ar * 20.f, ai * 20.f);
    auto r16 = __builtin_amdgcn_permlane16_swap(__float_as_uint(h[19].x), __float_as_uint(h[19].x), false, false);
    auto i16s = __builtin_amdgcn_permlane16_swap(__float_as_uint(h[19].y), __float_as_uint(h[19].y), false, false);
    auto r02 = __builtin_amdgcn_permlane32_swap(r16[0], r16[0], false, false);
    auto i02 = __builtin_amdgcn_permlane32_swap(i16s[0], i16s[0], false, false);
    auto r13 = __builtin_amdgcn_permlane32_swap(r16[1], r16[1], false, false);
    auto i13 = __builtin_amdgcn_permlane32_swap(i16s[1], i16s[1], false, false);
    const f2 e0 = {__uint_as_float(r02[0]), __uint_as_float(i02[0])}, e2 = {__uint_as_float(r02[1]), __uint_as_float(i02[1])};
    const f2 e1 = {__uint_as_float(r13[0]), __uint_as_float(i13[0])};
    const f2 p2 = cfma(e0, l20, e1), p3 = cfma(p2, l20, e2);
    pre = g == 0 ? (f2){0.f, 0.f} : g == 1 ? e0 : g == 2 ? p2 : p3;
  }
  f2 lt[20];
  lt[0] = cmul(coef2, lam);
#pragma unroll
  for (int j = 1; j < 20; ++j) lt[j] = cmul(lt[j - 1], lam);
#pragma unroll
  for (int j = 0; j < 20; ++j) h[j] = cmul(h[j], coef2);
  const float f0 = (float)(80 * ((c - 1 - g) & 3));
  const f2 wq = cexp(ar * f0, ai * f0), w320 = cexp(ar * 320.f, ai * 320.f);
  const float fg = (float)(20 * g);
  const f2 wg20 = cexp(ar * fg, ai * fg);
  tr4(cf[0][0][0], cf[0][0][1], cf[0][0][2], cf[0][0][3], qm);
  tr4(cf[0][1][0], cf[0][1][1], cf[0][1][2], cf[0][1][3], qm);
  tr4(cf[0][2][0], cf[0][2][1], cf[0][2][2], cf[0][2][3], qm);
  tr4(cf[0][3][0], cf[0][3][1], cf[0][3][2], cf[0][3][3], qm);
  FENCE();
  ST(4);

  f2 H = {0.f, 0.f};
  {
    const int kq = lane < 8 ? 8 * w + lane : 32 + w;
    const unsigned oq = WS_E + (unsigned)(c * 36 + kq) * 256;
    const unsigned oe = WS_E + (unsigned)(g * 36 + kap) * 256 + i16 * 16;
    v4u e[7];
#pragma unroll 1
    for (unsigned spin = 0; spin < SPIN_CAP; ++spin) {
      asm volatile("" ::: "memory");
      const v4u q = __builtin_amdgcn_raw_buffer_load_b128(rs, oq, 0, 16);
#pragma unroll
      for (int m = 0; m < 7; ++m) e[m] = __builtin_amdgcn_raw_buffer_load_b128(rs, g + 4 * m < c ? oe + m * (4 * 36 * 256) : oq, 0, 16);
      bool ok = q.y == tagA && q.w == tagB;
#pragma unroll
      for (int m = 0; m < 7; ++m) ok = ok && e[m].y == tagA && e[m].w == tagB;
      if (__all(ok)) break;
      __builtin_amdgcn_s_sleep(1);
    }
#pragma unroll
    for (int m = 0; m < 7; ++m) {
      const f2 nx = cfma(H, w320, (f2){__uint_as_float(e[m].x), __uint_as_float(e[m].z)});
      H = g + 4 * m < c ? nx : H;
    }
    H = cmul(H, wq);
    H = (f2){xsum_groups(H.x), xsum_groups(H.y)};
  }
  FENCE();
  ST(5);
  load_c(1, 0); load_c(1, 1); load_c(1, 2); load_c(1, 3);
#pragma unroll
  for (int a = 0; a < 4; ++a) c8[a] = ldg16(cbase, (unsigned)(16 * w) * 1152u, a * 1152 + 8 * 128);
  FENCE();
  {
    const f2 car = cfma(H, wg20, pre);
    LAS char* tb = sm + 10240 + w * 5376;
#pragma unroll
    for (int t = 0; t < 5; ++t) {
      f2 v[4];
#pragma unroll
      for (int r = 0; r < 4; ++r) v[r] = cfma(lt[4 * t + r], car, h[4 * t + r]);
      *(LAS v2u*)(tb + i16 * 168 + 40 * g + 8 * t) = (v2u){pk2(v[0].x, v[1].x), pk2(v[2].x, v[3].x)};
      *(LAS v2u*)(tb + (16 + i16) * 168 + 40 * g + 8 * t) = (v2u){pk2(-v[0].y, -v[1].y), pk2(-v[2].y, -v[3].y)};
    }
    asm volatile("" ::: "memory");
    const unsigned ra = (8 * g + (i16 >> 2)) * 168 + (i16 & 3) * 8;
#pragma unroll
    for (int t = 0; t < 5; ++t) {
      const v2u lo = ldstr(tb, ra + 32 * t), hi = ldstr(tb, ra + 4 * 168 + 32 * t);
      __builtin_amdgcn_raw_buffer_store_b128((v4u){lo.x, lo.y, hi.x, hi.y}, rs, hst, t * 36864, 16);
    }
  }
  FENCE();
  ST(6);
  tr4(cf[1][0][0], cf[1][0][1], cf[1][0][2], cf[1][0][3], qm);
  tr4(cf[1][1][0], cf[1][1][1], cf[1][1][2], cf[1][1][3], qm);
  tr4(cf[1][2][0], cf[1][2][1], cf[1][2][2], cf[1][2][3], qm);
  tr4(cf[1][3][0], cf[1][3][1], cf[1][3][2], cf[1][3][3], qm);
  tr4(c8[0], c8[1], c8[2], c8[3], qm);
  LAS char* c8s = sm + 32768;
#pragma unroll
  for (int a = 0; a < 4; ++a) *(LAS v4u*)(c8s + (a * 4 + w) * 1024 + lane * 16) = c8[a];
  const v2u dsk = *(const GAS v2u*)(p.DskA + 64 * s + 4 * i16);
  BARRIER();
  ST(7);

  const unsigned hld = WS_H + (unsigned)(c * 180 + 9 * w) * 1024 + lane * 16;
  v4u hf[2][9];
  auto load_h = [&](int t) {
#pragma unroll
    for (int i = 0; i < 9; ++i) hf[t & 1][i] = __builtin_amdgcn_raw_buffer_load_b128(rs, hld + (i & 3) * 1024, t * 36864 + (i >> 2) * 4096, 16);
  };
  auto bad_h = [&](int t) {
    unsigned m = 0;
#pragma unroll
    for (int i = 0; i < 9; ++i) { const v4u v = hf[t & 1][i]; m = max(max(m, v.x), v.y); m = max(max(m, v.z), v.w); }
    return __any(m == ~0u);
  };
  LAS char* red = sm + 49152;
  const float dk[4] = {bf2f(dsk.x & 0xffffu), bfhi(dsk.x), bf2f(dsk.y & 0xffffu), bfhi(dsk.y)};
  load_h(0); load_h(1);
  FENCE();
#pragma unroll
  for (int t = 0; t < 5; ++t) {
#pragma unroll 1
    for (unsigned spin = 0; spin < SPIN_CAP; ++spin) {
      if (!bad_h(t)) break;
      asm volatile("" ::: "memory");
      load_h(t);
    }
    FENCE();
    if (t == 0) ST(8);
    f4 acc[4];
#pragma unroll
    for (int n = 0; n < 4; ++n) acc[n] = mfma16(lds16(c8s, (w * 4 + n) * 1024 + lane * 16), hf[t & 1][8], (f4){0, 0, 0, 0});
#pragma unroll
    for (int i = 0; i < 8; ++i)
#pragma unroll
      for (int n = 0; n < 4; ++n) acc[n] = mfma16(cf[i >> 2][n][i & 3], hf[t & 1][i], acc[n]);
    FENCE();
    if (t + 2 < 5) load_h(t + 2);
    FENCE();
    LAS char* rb = (t & 1) ? sm + 10240 : red;
#pragma unroll
    for (int n = 0; n < 4; ++n) *(LAS f4*)(rb + w * 4352 + i16 * 272 + n * 64 + g * 16) = acc[n];
    BARRIER();
    {
      const int tr = 4 * w + g, tl = 16 * t + tr;
      f4 y = *(LAS f4*)(rb + tr * 272 + i16 * 16);
#pragma unroll
      for (int q = 1; q < 4; ++q) y += *(LAS f4*)(rb + q * 4352 + tr * 272 + i16 * 16);
      const int gq = tl / 20, rem = tl - 20 * gq, rho = 4 * gq + (rem & 3);
      const v2u xv = *(LAS v2u*)(sm + (rem >> 2) * 2048 + rho * 128 + (((i16 >> 1) ^ ((rho >> 1) & 3)) * 16) + (i16 & 1) * 8);
      const unsigned s01 = pk2(bf2f(xv.x & 0xffffu) * dk[0], bfhi(xv.x) * dk[1]), s23 = pk2(bf2f(xv.y & 0xffffu) * dk[2], bfhi(xv.y) * dk[3]);
      y += (f4){bf2f(s01 & 0xffffu), bfhi(s01), bf2f(s23 & 0xffffu), bfhi(s23)};
      const int row = c * TC + tl;
      if (row < L) *(GAS f4*)(p.out + (size_t)row * 1152 + 64 * s + 4 * i16) = y;
    }
    if (t == 0 && id == NA - NS && tid == 0)
      __hip_atomic_store((GAS unsigned*)(p.ws + WS_EPOCH_A), tagA, __ATOMIC_RELAXED, __HIP_MEMORY_SCOPE_AGENT);
    FENCE();
  }
  ST(9); STC(15);
  if (bid == PROBE_BID && w == 0) { const unsigned long long until = t0p + 4000 + 4 * (tpp - t0p); while (__builtin_amdgcn_s_memrealtime() < until) __builtin_amdgcn_s_sleep(2); }
}

DI void role_b(const Params& p, LAS char* sm, int b) {
  unsigned long long t0p = 0, tpp = 0, c0p = 0;
  const int tid = threadIdx.x, lane = tid & 63;
  const int w = __builtin_amdgcn_readfirstlane(tid >> 6);
  const int grp = b >> 4, r16 = b & 15, ch = 64 * grp + 4 * r16 + w;
  const int i16 = lane & 15, g = lane >> 4;
  const auto rs = __builtin_amdgcn_make_buffer_rsrc((void*)p.ws, 0, WS_TOTAL, 0x00020000);
  ST(0);
  constexpr unsigned WB = 13312;
  LAS char* kb = sm + w * WB;
  LAS char* ub = kb + 8448;
  LAS char* tile = sm + 4 * WB;

  const unsigned ep = ld_epoch(p.ws, WS_EPOCH_B + grp * 256);
  const unsigned rA = *(const GAS unsigned*)(p.lA + ch * NM + 2 * lane), rI = *(const GAS unsigned*)(p.Aim + ch * NM + 2 * lane);
  const unsigned rCr = *(const GAS unsigned*)(p.Cr + ch * NM + 2 * lane), rCi = *(const GAS unsigned*)(p.Ci + ch * NM + 2 * lane);
  const unsigned rdt = *(const GAS u16*)(p.ldtB + ch), rdk = *(const GAS u16*)(p.DskB + ch);
  v4u ut[4];
#pragma unroll
  for (int i = 0; i < 4; ++i) ut[i] = ldg16(p.u, (unsigned)(128 * r16 + 32 * w + 8 * i + (lane >> 3)) * 1152u + grp * 128u + (lane & 7) * 16u);
  FENCE();
  const unsigned tagA = ep + 1u, tagB = tag2(ep + 1u);
  if (lane < 30) *(LAS v4u*)(ub + lane * 16) = (v4u){0, 0, 0, 0};
#pragma unroll
  for (int i = 0; i < 4; ++i) *(LAS v4u*)(tile + (32 * w + 8 * i + (lane >> 3)) * 144 + (lane & 7) * 16) = ut[i];
  BARRIER();
  {
    const unsigned ra = (8 * g + (i16 >> 2)) * 144 + 32 * w + (i16 & 3) * 8;
    const unsigned so = WS_UT + (unsigned)((grp * 16 + 4 * w + (i16 >> 2)) * 16 + r16) * 1024 + g * 64 + (i16 & 3) * 16;
#pragma unroll
    for (int it = 0; it < 4; ++it) {
      const v2u lo = ldstr(tile, ra + it * 32 * 144), hi = ldstr(tile, ra + (it * 32 + 4) * 144);
      __builtin_amdgcn_raw_buffer_store_b128((v4u){lo.x, lo.y, hi.x, hi.y}, rs, so + it * 256, 0, 16);
    }
    asm volatile("s_waitcnt vmcnt(0)" ::: "memory");
    if (lane == 0) __builtin_amdgcn_raw_buffer_store_b64((v2u){tagA, tagB}, rs, WS_FLAG_U + (unsigned)((grp * 4 + w) * 16 + r16) * 8, 0, 16);
  }
  FENCE();
  ST(1);

  f4 kt[3];
  {
    const float dt = rbf(fexp(bf2f(rdt)));
    const unsigned ea = pk2(fexp(bf2f(rA & 0xffffu)), fexp(bfhi(rA)));
    float ar[2] = {-bf2f(ea & 0xffffu) * dt, -bfhi(ea) * dt};
    float ai[2] = {bf2f(rI & 0xffffu) * dt, bfhi(rI) * dt};
    f2 G[2] = {(f2){2.f * (bf2f(rCr & 0xffffu) * dt), 2.f * (bf2f(rCi & 0xffffu) * dt)}, (f2){2.f * (bfhi(rCr) * dt), 2.f * (bfhi(rCi) * dt)}};
    f2 z1[2], z2[2], z4[2], z8[2], z128[2];
#pragma unroll
    for (int m = 0; m < 2; ++m) {
      z1[m] = cexp(ar[m], ai[m]); z8[m] = cexp(8.f * ar[m], 8.f * ai[m]); z128[m] = cexp(128.f * ar[m], 128.f * ai[m]);
      z2[m] = cmul(z1[m], z1[m]); z4[m] = cmul(z2[m], z2[m]);
    }
    auto put = [&](int row, f2 a, f2 b2) { *(LAS v2u*)(kb + row * 528 + lane * 8) = (v2u){pk2(a.x, a.y), pk2(b2.x, b2.y)}; };
    const unsigned fr = i16 * 528 + g * 16;
    v4u rf[8];
    {
      f2 r0 = {1.f, 0.f}, r1 = {1.f, 0.f};
      const f2 c0 = {z8[0].x, -z8[0].y}, c1 = {z8[1].x, -z8[1].y};
#pragma unroll
      for (int j = 0; j < 16; ++j) { put(j, r0, r1); if (j < 15) { r0 = cmul(r0, c0); r1 = cmul(r1, c1); } }
      asm volatile("" ::: "memory");
#pragma unroll
      for (int k = 0; k < 8; ++k) rf[k] = lds16(kb, fr + k * 64);
      asm volatile("" ::: "memory");
    }
    auto tile_mma = [&](int t) {
      asm volatile("" ::: "memory");
      v4u lf[8];
#pragma unroll
      for (int k = 0; k < 8; ++k) lf[k] = lds16(kb, fr + k * 64);
      asm volatile("" ::: "memory");
      kt[t] = (f4){0, 0, 0, 0};
#pragma unroll
      for (int k = 0; k < 8; ++k) kt[t] = mfma16(lf[k], rf[k], kt[t]);
    };
#pragma unroll
    for (int a = 0; a < 2; ++a) {
      f2 v0 = G[0], v1 = G[1];
#pragma unroll
      for (int bb = 0; bb < 8; ++bb) { put(8 * a + bb, v0, v1); if (bb < 7) { v0 = cmul(v0, z1[0]); v1 = cmul(v1, z1[1]); } }
      G[0] = cmul(G[0], z128[0]); G[1] = cmul(G[1], z128[1]);
    }
    tile_mma(0);
#pragma unroll
    for (int a = 2; a < 4; ++a) {
      f2 v0 = G[0], v1 = G[1];
#pragma unroll
      for (int bb = 0; bb < 4; ++bb) { put(4 * (a - 2) + bb, v0, v1); if (bb < 3) { v0 = cmul(v0, z2[0]); v1 = cmul(v1, z2[1]); } }
      G[0] = cmul(G[0], z128[0]); G[1] = cmul(G[1], z128[1]);
    }
#pragma unroll
    for (int a = 4; a < 8; ++a) {
      put(8 + 2 * (a - 4), G[0], G[1]);
      put(9 + 2 * (a - 4), cmul(G[0], z4[0]), cmul(G[1], z4[1]));
      G[0] = cmul(G[0], z128[0]); G[1] = cmul(G[1], z128[1]);
    }
    tile_mma(1);
#pragma unroll
    for (int a = 8; a < 17; ++a) {
      put(a - 8, G[0], G[1]);
      if (a < 16) { G[0] = cmul(G[0], z128[0]); G[1] = cmul(G[1], z128[1]); }
    }
    tile_mma(2);
  }
  FENCE();
  {
    LAS u16* k0 = (LAS u16*)kb;
    {
      unsigned kv[4], pv[4];
#pragma unroll
      for (int r = 0; r < 4; ++r) { kv[r] = pk2(kt[2][r], kt[2][r]); pv[r] = __builtin_amdgcn_update_dpp(0u, kv[r], 0x111, 0xf, 0xf, true); }
      if (g < 2) {
#pragma unroll
        for (int r = 0; r < 4; ++r) {
          const int p0 = 128 * (8 + 4 * g + r) + 8 * i16;
          const unsigned last = (i16 & 1) ? ((kv[r] & 0xffffu) | (pv[r] & 0xffff0000u)) : kv[r];
          *(LAS v2u*)(k0 + 2044 - p0) = (v2u){kv[r], kv[r]};
          *(LAS v2u*)(k0 + 2048 - p0) = (v2u){kv[r], last};
        }
      }
      if (lane == 32) *(LAS v2u*)(k0) = (v2u){kv[0], kv[0]};
    }
    asm volatile("" ::: "memory");
    if (g >= 2) {
#pragma unroll
      for (int q = 0; q < 2; ++q) {
        const int p0 = 128 * (4 + 2 * (g - 2) + q) + 8 * i16;
        const float a0 = kt[1][2 * q], a1 = kt[1][2 * q + 1];
        *(LAS unsigned*)(k0 + 2042 - p0) = pk2(a1, a1);
        *(LAS v2u*)(k0 + 2044 - p0) = (v2u){pk2(a1, a0), pk2(a0, a0)};
        *(LAS unsigned*)(k0 + 2048 - p0) = pk2(a0, a0);
      }
    }
    asm volatile("" ::: "memory");
    if (g < 2) {
      const int p0 = 128 * (2 + g) + 8 * i16;
      const unsigned k3 = pk2(kt[1][3], kt[1][0]);
      k0[2041 - p0] = (u16)k3;
      *(LAS unsigned*)(k0 + 2042 - p0) = pk2(kt[1][2], kt[1][2]);
      *(LAS v2u*)(k0 + 2044 - p0) = (v2u){pk2(kt[1][2], kt[1][1]), pk2(kt[1][0], kt[1][0])};
      k0[2048 - p0] = (u16)(k3 >> 16);
    }
    asm volatile("" ::: "memory");
    {
      const int l0 = 128 * (g >> 1) + 8 * i16 + 4 * (g & 1);
      *(LAS v2u*)(k0 + 2044 - l0) = (v2u){pk2(kt[0][3], kt[0][2]), pk2(kt[0][1], kt[0][0])};
    }
    if (lane < 3) *(LAS v4u*)(k0 + 2048 + 8 * lane) = (v4u){0, 0, 0, 0};
    asm volatile("" ::: "memory");
#pragma unroll
    for (int it = 0; it < 5; ++it) {
      const int e = it * 64 + lane;
      if (e < 258) {
        const v4u a = lds16(kb, e * 16);
        const unsigned nx = *(LAS unsigned*)(kb + e * 16 + 16);
        *(LAS v4u*)(kb + 4160 + e * 16) = (v4u){__builtin_amdgcn_alignbyte(a.y, a.x, 2), __builtin_amdgcn_alignbyte(a.z, a.y, 2),
                                                __builtin_amdgcn_alignbyte(a.w, a.z, 2), __builtin_amdgcn_alignbyte(nx, a.w, 2)};
      }
    }
  }
  FENCE();
  ST(2);
  {
    const unsigned fo = WS_FLAG_U + (unsigned)((grp * 4 + (r16 >> 2)) * 16 + i16) * 8;
#pragma unroll 1
    for (unsigned spin = 0; spin < SPIN_CAP; ++spin) {
      asm volatile("" ::: "memory");
      const v2u f = __builtin_amdgcn_raw_buffer_load_b64(rs, fo, 0, 16);
      if (__all(f.x == tagA && f.y == tagB)) break;
      __builtin_amdgcn_s_sleep(2);
    }
    FENCE();
#pragma unroll
    for (int i = 0; i < 4; ++i) ut[i] = __builtin_amdgcn_raw_buffer_load_b128(rs, WS_UT + (unsigned)((grp * 16 + r16) * 16 + 4 * w + i) * 1024 + lane * 16, 0, 16);
#pragma unroll
    for (int i = 0; i < 4; ++i) *(LAS v4u*)(sm + (lane & 3) * WB + 8448 + 480 + (4 * w + i) * 256 + (lane >> 2) * 16) = ut[i];
  }
  BARRIER();
  ST(3);
  f4 acc[8];
#pragma unroll
  for (int t = 0; t < 8; ++t) acc[t] = (f4){0, 0, 0, 0};
  {
    const int ab = (i16 & 1) ? (2031 - i16 + 8 * g) * 2 : 4160 + (2030 - i16 + 8 * g) * 2;
    const int bb = 448 + 32 * i16 + 16 * g;
#pragma unroll 1
    for (int r = 0; r < 8; ++r) {
      v4u A[8], B[8];
#pragma unroll
      for (int q = 0; q < 8; ++q) {
        LAS unsigned* pa = (LAS unsigned*)(kb + ab - 64 * r - 512 * q);
        A[q] = (v4u){pa[0], pa[1], pa[2], pa[3]};
      }
#pragma unroll
      for (int e = 0; e < 8; ++e) B[e] = lds16(ub, bb - 64 * r + 512 * e);
#pragma unroll
      for (int q = 0; q < 8; ++q)
#pragma unroll
        for (int e = 0; e + q < 8; ++e) acc[e + q] = mfma16(A[q], B[e], acc[e + q]);
    }
  }
  FENCE();
  ST(4);
  {
    const float dk = bf2f(rdk);
    const unsigned so = WS_YT + (unsigned)(((grp * 16 + (i16 >> 3)) * 16 + r16) * 4 + w) * 256 + g * 64 + (i16 & 7) * 8;
#pragma unroll
    for (int t = 0; t < 8; ++t) {
      const v2u uv = *(LAS v2u*)(ub + 480 + (256 * t + 16 * i16 + 4 * g) * 2);
      const unsigned y01 = pk2(acc[t][0], acc[t][1]), y23 = pk2(acc[t][2], acc[t][3]);
      const unsigned s01 = pk2(bf2f(uv.x & 0xffffu) * dk, bfhi(uv.x) * dk), s23 = pk2(bf2f(uv.y & 0xffffu) * dk, bfhi(uv.y) * dk);
      const v2u o = {pk2(bf2f(y01 & 0xffffu) + bf2f(s01 & 0xffffu), bfhi(y01) + bfhi(s01)),
                     pk2(bf2f(y23 & 0xffffu) + bf2f(s23 & 0xffffu), bfhi(y23) + bfhi(s23))};
      __builtin_amdgcn_raw_buffer_store_b64(o, rs, so + t * (2 * 16 * 1024), 0, 16);
    }
    asm volatile("s_waitcnt vmcnt(0)" ::: "memory");
    if (lane == 0) __builtin_amdgcn_raw_buffer_store_b64((v2u){tagA, tagB}, rs, WS_FLAG_Y + (unsigned)(grp * 64 + 4 * r16 + w) * 8, 0, 16);
  }
  FENCE();
  ST(5);
  {
    const unsigned fo = WS_FLAG_Y + (unsigned)(grp * 64 + 16 * w + i16) * 8;
#pragma unroll 1
    for (unsigned spin = 0; spin < SPIN_CAP; ++spin) {
      asm volatile("" ::: "memory");
      const v2u f = __builtin_amdgcn_raw_buffer_load_b64(rs, fo, 0, 16);
      if (__all(f.x == tagA && f.y == tagB)) break;
      __builtin_amdgcn_s_sleep(2);
    }
    FENCE();
#pragma unroll
    for (int i = 0; i < 4; ++i) ut[i] = __builtin_amdgcn_raw_buffer_load_b128(rs, WS_YT + (unsigned)((grp * 16 + r16) * 16 + 4 * w + i) * 1024 + lane * 16, 0, 16);
    LAS u16* tp = (LAS u16*)(tile + (32 * (i16 & 3) + 4 * (i16 >> 2)) * 144) + 16 * w + g;
#pragma unroll
    for (int i = 0; i < 4; ++i) {
#pragma unroll
      for (int k = 0; k < 4; ++k) {
        const int row = 16 * (k >> 1) + 2 * (k & 1);
        tp[row * 72 + 4 * i] = (u16)ut[i][k];
        tp[(row + 1) * 72 + 4 * i] = (u16)(ut[i][k] >> 16);
      }
    }
  }
  BARRIER();
  if (r16 == 0 && tid == 0)
    __hip_atomic_store((GAS unsigned*)(p.ws + WS_EPOCH_B + grp * 256), tagA, __ATOMIC_RELAXED, __HIP_MEMORY_SCOPE_AGENT);
#pragma unroll
  for (int i = 0; i < 8; ++i) {
    const int row = 32 * w + 4 * i + g;
    const v2u v = *(LAS v2u*)(tile + row * 144 + i16 * 8);
    *(GAS v4u*)(p.out + (size_t)(128 * r16 + row) * 1152 + 576 + 64 * grp + 4 * i16) = (v4u){v.x << 16, v.x & 0xffff0000u, v.y << 16, v.y & 0xffff0000u};
  }
  ST(6);
}

__global__ void __launch_bounds__(256, 2) ssm_fused(Params p) {
  extern __shared__ __attribute__((aligned(16))) char smem[];
  LAS char* sm = (LAS char*)smem;
  const int bid = blockIdx.x;
  unsigned long long pc;
  asm volatile("s_getpc_b64 %0" : "=s"(pc));
  pc &= ~255ull;
  const unsigned part = (((unsigned)bid >> 3) * 4 + (threadIdx.x >> 6)) % 5u;
  unsigned off = part * 8192u + (threadIdx.x & 63) * 128u;
  off = off < CODE_BYTES ? off : 0;
  const unsigned pf = *(const GAS unsigned*)((const GAS char*)pc + off);
  if (bid < NA) role_a(p, sm, bid);
  else role_b(p, sm, bid - NA);
  asm volatile("" :: "v"(pf));
}

extern "C" void kernel_launch(void* const* d_in, const int* in_sizes, int n_in,
                              void* d_out, int out_size, void* d_ws, size_t ws_size,
                              hipStream_t stream) {
  Params p;
  p.x = (const u16*)d_in[0]; p.Lre = (const u16*)d_in[1]; p.Lim = (const u16*)d_in[2];
  p.Bre = (const u16*)d_in[3]; p.Bim = (const u16*)d_in[4]; p.Cre = (const u16*)d_in[5]; p.Cim = (const u16*)d_in[6];
  p.ldtA = (const u16*)d_in[7]; p.DskA = (const u16*)d_in[8];
  p.u = (const u16*)d_in[9]; p.lA = (const u16*)d_in[10]; p.Aim = (const u16*)d_in[11];
  p.Cr = (const u16*)d_in[12]; p.Ci = (const u16*)d_in[13]; p.ldtB = (const u16*)d_in[14]; p.DskB = (const u16*)d_in[15];
  p.out = (float*)d_out; p.ws = (char*)d_ws;
  (void)hipFuncSetAttribute((const void*)ssm_fused, hipFuncAttributeMaxDynamicSharedMemorySize, LDS_BYTES);
  ssm_fused<<<NA + GRID_B, 256, LDS_BYTES, stream>>>(p);
}
```

```cpp
#define OUT_AUX 17
#include <hip/hip_runtime.h>
#include <hip/hip_bf16.h>
#include <stdint.h>

typedef unsigned short u16;
typedef unsigned v4u __attribute__((ext_vector_type(4)));
typedef unsigned v2u __attribute__((ext_vector_type(2)));
typedef float f2 __attribute__((ext_vector_type(2)));
typedef float f4 __attribute__((ext_vector_type(4)));
typedef __bf16 bf2 __attribute__((ext_vector_type(2)));
typedef __bf16 bf8 __attribute__((ext_vector_type(8)));
typedef short s4 __attribute__((ext_vector_type(4)));
#define GAS __attribute__((address_space(1)))
#define LAS __attribute__((address_space(3)))
#define DI __device__ __forceinline__

constexpr int L = 2048, D = 576, NM = 128;
constexpr int TC = 80, NCH = 26, NS = 9;
constexpr int NA = NCH * NS;
constexpr int NB = 144;
constexpr int LDS_BYTES = 81920;

constexpr unsigned WS_EPOCH_A = 0;
constexpr unsigned WS_EPOCH_B = 256;
constexpr unsigned WS_FLAG_U = 4096;
constexpr unsigned WS_FLAG_Y = 4096 + 8192;
constexpr unsigned WS_E = 32768;
constexpr unsigned WS_E_BYTES = NCH * 36 * 256;
constexpr unsigned WS_H = WS_E + 262144;
constexpr unsigned WS_H_BYTES = NCH * 5 * 36 * 1024;
constexpr unsigned WS_UT = WS_H + WS_H_BYTES;
constexpr unsigned WS_UT_BYTES = 9 * 16 * 16 * 1024;
constexpr unsigned WS_YT = WS_UT + WS_UT_BYTES;
constexpr unsigned WS_YT_BYTES = 9 * 16 * 16 * 1024;
constexpr unsigned WS_TOTAL = WS_YT + WS_YT_BYTES;
#define STC(i) do {} while (0)
#define ST(i) do {} while (0)

#define NPOISON 5
#define GRID_B NB
constexpr unsigned CODE_BYTES = 39000;
constexpr unsigned SPIN_CAP = 1u << 17;

struct Params {
  const u16 *x, *Lre, *Lim, *Bre, *Bim, *Cre, *Cim, *ldtA, *DskA;
  const u16 *u, *lA, *Aim, *Cr, *Ci, *ldtB, *DskB;
  float* out;
  char* ws;
};

DI float bf2f(unsigned v) { return __uint_as_float(v << 16); }
DI float bfhi(unsigned v) { return __uint_as_float(v & 0xffff0000u); }
DI unsigned pk2(float a, float b) { f2 z = {a, b}; return __builtin_bit_cast(unsigned, __builtin_convertvector(z, bf2)); }
DI float rbf(float f) { return bf2f(pk2(f, 0.f) & 0xffffu); }
DI f2 cmul(f2 a, f2 b) {
  f2 t = a * (f2){b.x, b.x};
  return __builtin_elementwise_fma((f2){a.y, a.x}, (f2){-b.y, b.y}, t);
}
DI f2 cfma(f2 a, f2 b, f2 c) {
  f2 t = __builtin_elementwise_fma(a, (f2){b.x, b.x}, c);
  return __builtin_elementwise_fma((f2){a.y, a.x}, (f2){-b.y, b.y}, t);
}
DI float fexp(float x) {
  const float c_hi = 1.44269502162933349609375f, c_lo = 1.925963033500011e-8f;
  float t = x * c_hi;
  float e = __builtin_fmaf(x, c_hi, -t);
  e = __builtin_fmaf(x, c_lo, e);
  float r = __builtin_amdgcn_exp2f(t);
  return __builtin_fmaf(r * e, 0.693147180559945f, r);
}
DI f2 cexp(float re, float im) {
  const float r_hi = 0.15915494f, r_lo = 6.4206382e-9f;
  float m = fexp(re);
  float t = im * r_hi;
  float e = __builtin_fmaf(im, r_hi, -t);
  e = __builtin_fmaf(im, r_lo, e);
  float f = __builtin_amdgcn_fractf(t) + e;
  return (f2){m * __builtin_amdgcn_cosf(f), m * __builtin_amdgcn_sinf(f)};
}
DI f4 mfma16(v4u a, v4u b, f4 c) {
  return __builtin_amdgcn_mfma_f32_16x16x32_bf16(__builtin_bit_cast(bf8, a), __builtin_bit_cast(bf8, b), c, 0, 0, 0);
}
DI v4u ldg16(const void* base, unsigned off, int cst = 0) { return *(const GAS v4u*)((const GAS char*)base + off + cst); }
DI v4u lds16(LAS char* s, unsigned off) { return *(LAS v4u*)(s + off); }
DI v2u ldstr(LAS char* s, unsigned off) {
  return __builtin_bit_cast(v2u, __builtin_amdgcn_ds_read_tr16_b64_v4i16((LAS s4*)(s + off)));
}
#define FENCE() do { __builtin_amdgcn_sched_barrier(0); asm volatile("" ::: "memory"); } while (0)
#define BARRIER() do { __builtin_amdgcn_sched_barrier(0); asm volatile("s_waitcnt lgkmcnt(0)\n\ts_barrier" ::: "memory"); __builtin_amdgcn_sched_barrier(0); } while (0)

struct QMask { unsigned long long e1, o1, e2, o2; };
#define TRC(d, s0, s1, P) "v_cndmask_b32_dpp " d ", " s0 ", " s1 ", vcc quad_perm:" P " row_mask:0xf bank_mask:0xf\n\t"
DI void tr4half(v2u& a0, v2u& a1, v2u& a2, v2u& a3, const QMask& m) {
  unsigned a00 = a0.x, a01 = a0.y, a10 = a1.x, a11 = a1.y, a20 = a2.x, a21 = a2.y, a30 = a3.x, a31 = a3.y;
  unsigned t00, t01, t20, t21;
  asm volatile(
      "s_mov_b64 vcc, %12\n\ts_nop 0\n\t"
      TRC("%8", "%2", "%0", "[1,0,3,2]") TRC("%9", "%3", "%1", "[1,0,3,2]")
      TRC("%10", "%6", "%4", "[1,0,3,2]") TRC("%11", "%7", "%5", "[1,0,3,2]")
      "s_mov_b64 vcc, %13\n\t"
      TRC("%2", "%0", "%2", "[1,0,3,2]") TRC("%3", "%1", "%3", "[1,0,3,2]")
      TRC("%6", "%4", "%6", "[1,0,3,2]") TRC("%7", "%5", "%7", "[1,0,3,2]")
      "s_mov_b64 vcc, %14\n\t"
      TRC("%0", "%10", "%8", "[2,3,0,1]") TRC("%1", "%11", "%9", "[2,3,0,1]")
      TRC("%4", "%6", "%2", "[2,3,0,1]") TRC("%5", "%7", "%3", "[2,3,0,1]")
      "s_mov_b64 vcc, %15\n\t"
      TRC("%10", "%8", "%10", "[2,3,0,1]") TRC("%11", "%9", "%11", "[2,3,0,1]")
      TRC("%6", "%2", "%6", "[2,3,0,1]") TRC("%7", "%3", "%7", "[2,3,0,1]")
      "s_nop 0"
      : "+v"(a00), "+v"(a01), "+v"(a10), "+v"(a11), "+v"(a20), "+v"(a21), "+v"(a30), "+v"(a31),
        "=&v"(t00), "=&v"(t01), "=&v"(t20), "=&v"(t21)
      : "s"(m.e1), "s"(m.o1), "s"(m.e2), "s"(m.o2)
      : "vcc");
  a0 = (v2u){a00, a01}; a1 = (v2u){a20, a21}; a2 = (v2u){t20, t21}; a3 = (v2u){a30, a31};
}
DI void tr4(v4u& a0, v4u& a1, v4u& a2, v4u& a3, const QMask& m) {
  v2u l0 = a0.xy, l1 = a1.xy, l2 = a2.xy, l3 = a3.xy, h0 = a0.zw, h1 = a1.zw, h2 = a2.zw, h3 = a3.zw;
  tr4half(l0, l1, l2, l3, m);
  tr4half(h0, h1, h2, h3, m);
  a0 = (v4u){l0.x, l0.y, h0.x, h0.y}; a1 = (v4u){l1.x, l1.y, h1.x, h1.y};
  a2 = (v4u){l2.x, l2.y, h2.x, h2.y}; a3 = (v4u){l3.x, l3.y, h3.x, h3.y};
}
DI float xsum_groups(float v) {
  auto a = __builtin_amdgcn_permlane16_swap(__float_as_uint(v), __float_as_uint(v), false, false);
  v = __uint_as_float(a[0]) + __uint_as_float(a[1]);
  auto b = __builtin_amdgcn_permlane32_swap(__float_as_uint(v), __float_as_uint(v), false, false);
  return __uint_as_float(b[0]) + __uint_as_float(b[1]);
}
DI unsigned tag2(unsigned t) { return (t * 0x9E3779B1u) ^ 0x5bd1e995u; }
DI unsigned ld_epoch(const char* ws, unsigned off) {
  return __hip_atomic_load((const GAS unsigned*)(ws + off), __ATOMIC_RELAXED, __HIP_MEMORY_SCOPE_AGENT);
}

DI void role_a(const Params& p, LAS char* sm, int bid) {
  const int tid = threadIdx.x, lane = tid & 63;
  const int w = __builtin_amdgcn_readfirstlane(tid >> 6);
  int id; { const int q = NA / 8, r = NA % 8, xcd = bid & 7, off = bid >> 3; id = (xcd < r ? xcd * (q + 1) : r * (q + 1) + (xcd - r) * q) + off; }
  const int c = id / NS, s = id % NS;
  const int i16 = lane & 15, g = lane >> 4, c0 = lane & 3, c1 = (lane >> 2) & 3;
  const QMask qm = {0x5555555555555555ull, 0xAAAAAAAAAAAAAAAAull, 0x3333333333333333ull, 0xCCCCCCCCCCCCCCCCull};
  const auto rs = __builtin_amdgcn_make_buffer_rsrc((void*)p.ws, 0, WS_TOTAL, 0x00020000);
  const auto ro = __builtin_amdgcn_make_buffer_rsrc((void*)p.out, 0, L * 4608, 0x00020000);
  __builtin_amdgcn_s_setprio(2);
  ST(0); STC(14);

  const unsigned ep = ld_epoch(p.ws, WS_EPOCH_A);
  const int pl = 64 * s + 8 * w + (i16 & 7) + 32 * (i16 >> 3);
  const unsigned rLre = *(const GAS u16*)(p.Lre + pl), rLim = *(const GAS u16*)(p.Lim + pl), rLdt = *(const GAS u16*)(p.ldtA + pl);
  const int kap = 4 * s + w;
  const unsigned hslot = kap < 32 ? 9 * (kap >> 3) + (kap & 7) : 9 * (kap - 32) + 8;
  const unsigned hst = WS_H + (unsigned)(c * 180 + hslot) * 1024 + lane * 16;
  {
    const v4u pz = {~0u, ~0u, ~0u, ~0u};
#pragma unroll
    for (int t = 0; t < NPOISON; ++t) __builtin_amdgcn_raw_buffer_store_b128(pz, rs, hst, t * 36864, 16);
  }
  FENCE();
  auto kb_of = [&](int ord) { int k = s + 1 + ord; return k >= 9 ? k - 9 : k; };
  const auto rx = __builtin_amdgcn_make_buffer_rsrc((void*)p.x, 0, L * D * 2, 0x00020000);
  const auto rbr = __builtin_amdgcn_make_buffer_rsrc((void*)p.Bre, 0, D * D * 2, 0x00020000);
  const auto rbi = __builtin_amdgcn_make_buffer_rsrc((void*)p.Bim, 0, D * D * 2, 0x00020000);
  const unsigned xv0 = (unsigned)(20 * (lane >> 5) + ((lane >> 3) & 3)) * 1152u + (lane & 7) * 16u;
  auto ldx = [&](int ord, int sub) {
    return __builtin_amdgcn_raw_buffer_load_b128(rx, xv0, (c * TC + 40 * (sub & 1) + 4 * (sub >> 1)) * 1152 + kb_of(ord) * 128, 0);
  };
  const unsigned xw = (lane >> 3) * 128 + (((lane & 7) ^ (lane >> 4)) * 16);
  LAS char* xwb = sm + xw + (w >> 1) * 10240 + (w & 1) * 5120;
  const unsigned brow = (unsigned)(4 * (c1 & 1) + 32 * (c1 >> 1)) * 1152u;
  const int bso = (64 * s + 8 * w) * 1152;

  v4u xs[5][5], br[4][4], bi[4][4], bm[4];
  auto load_x = [&](int G) {
#pragma unroll
    for (int i = 0; i < 5; ++i) xs[G][i] = ldx(2 * G + (w >> 1), 5 * (w & 1) + i);
  };
  auto load_b = [&](int G) {
    const unsigned o = brow + (unsigned)(g >> 1 ? kb_of(2 * G + 1) : kb_of(2 * G)) * 128u + (4 * (g & 1) + c0) * 16u;
#pragma unroll
    for (int a = 0; a < 4; ++a) br[G][a] = __builtin_amdgcn_raw_buffer_load_b128(rbr, o + a * 1152, bso, 0);
#pragma unroll
    for (int a = 0; a < 4; ++a) bi[G][a] = __builtin_amdgcn_raw_buffer_load_b128(rbi, o + a * 1152, bso, 0);
  };
  auto store_x = [&](int G) {
#pragma unroll
    for (int i = 0; i < 5; ++i) *(LAS v4u*)(xwb + G * 20480 + i * 1024) = xs[G][i];
  };
  load_x(0); load_b(0); load_x(1); load_b(1); load_x(2); load_b(2);
  FENCE();
  ST(10);

  const float dt = rbf(fexp(bf2f(rLdt)));
  const float lre = bf2f(rLre), lim = bf2f(rLim);
  const float ar = dt * lre, ai = dt * lim;
  const f2 lam = cexp(ar, ai);
  f2 coef2;
  { const float nr = lam.x - 1.0f, ni = lam.y, den = 2.0f / (lre * lre + lim * lim);
    coef2 = (f2){(nr * lre + ni * lim) * den, (ni * lre - nr * lim) * den}; }
  const float fe = (float)(20 * (3 - g));
  const f2 wend = cexp(ar * fe, ai * fe);
  const unsigned tagA = ep + 1u, tagB = tag2(ep + 1u);
  FENCE();
  ST(11);

  f4 aR[5], aI[5];
#pragma unroll
  for (int t = 0; t < 5; ++t) { aR[t] = (f4){0, 0, 0, 0}; aI[t] = (f4){0, 0, 0, 0}; }
  const unsigned fsw = (i16 >> 1) & 3;
  const unsigned ard = (g >> 1) * 10240 + i16 * 128 + (g & 1) * 64;
  auto mma_group = [&](int G) {
    tr4(br[G][0], br[G][1], br[G][2], br[G][3], qm);
    tr4(bi[G][0], bi[G][1], bi[G][2], bi[G][3], qm);
#pragma unroll
    for (int k = 0; k < 4; ++k) {
      v4u xa[5];
#pragma unroll
      for (int t = 0; t < 5; ++t) xa[t] = lds16(sm, G * 20480 + t * 2048 + ard + ((k ^ fsw) * 16));
#pragma unroll
      for (int t = 0; t < 5; ++t) { aR[t] = mfma16(xa[t], br[G][k], aR[t]); aI[t] = mfma16(xa[t], bi[G][k], aI[t]); }
    }
  };
  v4u cf[2][4][4], c8[4];
  const char* cbase = (const char*)(g >> 1 ? p.Cim : p.Cre) + (unsigned)(64 * s + 4 * c1) * 1152u + (32 * (g & 1) + 8 * c0) * 2u;
  auto load_c = [&](int b, int n) {
#pragma unroll
    for (int a = 0; a < 4; ++a) cf[b][n][a] = ldg16(cbase, (unsigned)(16 * n * 1152 + 2 * w * 128), a * 1152 + b * 128);
  };

  store_x(0);
  BARRIER();
  ST(1);
  mma_group(0);
  FENCE();
  ST(12);
  load_x(3);
  load_b(3);
  FENCE();
  store_x(1); BARRIER();
  mma_group(1);
  FENCE();
#pragma unroll
  for (int i = 0; i < 3; ++i) { int sub = w + 4 * i; sub = sub < 10 ? sub : 9; xs[4][i] = ldx(8, sub); }
  {
    const char* mb = (const char*)(c0 >> 1 ? p.Bim : p.Bre);
    const unsigned o = bso + brow + (unsigned)kb_of(8) * 128u + (4 * (g & 1) + 2 * (g >> 1) + (c0 & 1)) * 16u;
#pragma unroll
    for (int a = 0; a < 4; ++a) bm[a] = ldg16(mb, o, a * 1152);
  }
  load_c(0, 0);
  FENCE();
  store_x(2);
  BARRIER();
  mma_group(2);
  FENCE();
  load_c(0, 1); load_c(0, 2);
  FENCE();
  store_x(3);
#pragma unroll
  for (int i = 0; i < 3; ++i) { int sub = w + 4 * i; sub = sub < 10 ? sub : 9; *(LAS v4u*)(sm + sub * 1024 + xw) = xs[4][i]; }
  BARRIER();
  mma_group(3);
  FENCE();
  load_c(0, 3);
  FENCE();
  {
    tr4(bm[0], bm[1], bm[2], bm[3], qm);
    const unsigned a4 = i16 * 128;
#pragma unroll
    for (int k = 0; k < 2; ++k) {
      v4u xa[5];
#pragma unroll
      for (int t = 0; t < 5; ++t) xa[t] = lds16(sm, t * 2048 + a4 + (((4 * (g & 1) + 2 * (g >> 1) + k) ^ fsw) * 16));
#pragma unroll
      for (int t = 0; t < 5; ++t) { aR[t] = mfma16(xa[t], bm[k], aR[t]); aI[t] = mfma16(xa[t], bm[2 + k], aI[t]); }
    }
  }
  FENCE();
  ST(2);

  f2 h[20];
  h[0] = (f2){aR[0][0], aI[0][0]};
#pragma unroll
  for (int j = 1; j < 20; ++j) h[j] = cfma(h[j - 1], lam, (f2){aR[j >> 2][j & 3], aI[j >> 2][j & 3]});
  {
    const f2 e = cmul(h[19], wend);
    const float er = xsum_groups(e.x), ei = xsum_groups(e.y);
    const v4u gr = {__float_as_uint(er), tagA, __float_as_uint(ei), tagB};
    if (g == 0) __builtin_amdgcn_raw_buffer_store_b128(gr, rs, WS_E + (unsigned)(c * 36 + kap) * 256 + i16 * 16, 0, 16);
  }
  FENCE();
  ST(3);
  f2 pre;
  {
    const f2 l20 = cexp(ar * 20.f, ai * 20.f);
    auto r16 = __builtin_amdgcn_permlane16_swap(__float_as_uint(h[19].x), __float_as_uint(h[19].x), false, false);
    auto i16s = __builtin_amdgcn_permlane16_swap(__float_as_uint(h[19].y), __float_as_uint(h[19].y), false, false);
    auto r02 = __builtin_amdgcn_permlane32_swap(r16[0], r16[0], false, false);
    auto i02 = __builtin_amdgcn_permlane32_swap(i16s[0], i16s[0], false, false);
    auto r13 = __builtin_amdgcn_permlane32_swap(r16[1], r16[1], false, false);
    auto i13 = __builtin_amdgcn_permlane32_swap(i16s[1], i16s[1], false, false);
    const f2 e0 = {__uint_as_float(r02[0]), __uint_as_float(i02[0])}, e2 = {__uint_as_float(r02[1]), __uint_as_float(i02[1])};
    const f2 e1 = {__uint_as_float(r13[0]), __uint_as_float(i13[0])};
    const f2 p2 = cfma(e0, l20, e1), p3 = cfma(p2, l20, e2);
    pre = g == 0 ? (f2){0.f, 0.f} : g == 1 ? e0 : g == 2 ? p2 : p3;
  }
  f2 lt[20];
  lt[0] = cmul(coef2, lam);
#pragma unroll
  for (int j = 1; j < 20; ++j) lt[j] = cmul(lt[j - 1], lam);
#pragma unroll
  for (int j = 0; j < 20; ++j) h[j] = cmul(h[j], coef2);
  const float f0 = (float)(80 * ((c - 1 - g) & 3));
  const f2 wq = cexp(ar * f0, ai * f0), w320 = cexp(ar * 320.f, ai * 320.f);
  const float fg = (float)(20 * g);
  const f2 wg20 = cexp(ar * fg, ai * fg);
  tr4(cf[0][0][0], cf[0][0][1], cf[0][0][2], cf[0][0][3], qm);
  tr4(cf[0][1][0], cf[0][1][1], cf[0][1][2], cf[0][1][3], qm);
  tr4(cf[0][2][0], cf[0][2][1], cf[0][2][2], cf[0][2][3], qm);
  tr4(cf[0][3][0], cf[0][3][1], cf[0][3][2], cf[0][3][3], qm);
  FENCE();
  ST(4);

  f2 H = {0.f, 0.f};
  {
    const int kq = lane < 8 ? 8 * w + lane : 32 + w;
    const unsigned oq = WS_E + (unsigned)(c * 36 + kq) * 256;
    const unsigned oe = WS_E + (unsigned)(g * 36 + kap) * 256 + i16 * 16;
    v4u e[7];
#pragma unroll 1
    for (unsigned spin = 0; spin < SPIN_CAP; ++spin) {
      asm volatile("" ::: "memory");
      const v4u q = __builtin_amdgcn_raw_buffer_load_b128(rs, oq, 0, 16);
#pragma unroll
      for (int m = 0; m < 7; ++m) e[m] = __builtin_amdgcn_raw_buffer_load_b128(rs, g + 4 * m < c ? oe + m * (4 * 36 * 256) : oq, 0, 16);
      bool ok = q.y == tagA && q.w == tagB;
#pragma unroll
      for (int m = 0; m < 7; ++m) ok = ok && e[m].y == tagA && e[m].w == tagB;
      if (__all(ok)) break;
      __builtin_amdgcn_s_sleep(1);
    }
#pragma unroll
    for (int m = 0; m < 7; ++m) {
      const f2 nx = cfma(H, w320, (f2){__uint_as_float(e[m].x), __uint_as_float(e[m].z)});
      H = g + 4 * m < c ? nx : H;
    }
    H = cmul(H, wq);
    H = (f2){xsum_groups(H.x), xsum_groups(H.y)};
  }
  FENCE();
  ST(5);
  load_c(1, 0); load_c(1, 1); load_c(1, 2); load_c(1, 3);
#pragma unroll
  for (int a = 0; a < 4; ++a) c8[a] = ldg16(cbase, (unsigned)(16 * w) * 1152u, a * 1152 + 8 * 128);
  FENCE();
  {
    const f2 car = cfma(H, wg20, pre);
    LAS char* tb = sm + 10240 + w * 5376;
#pragma unroll
    for (int t = 0; t < 5; ++t) {
      f2 v[4];
#pragma unroll
      for (int r = 0; r < 4; ++r) v[r] = cfma(lt[4 * t + r], car, h[4 * t + r]);
      *(LAS v2u*)(tb + i16 * 168 + 40 * g + 8 * t) = (v2u){pk2(v[0].x, v[1].x), pk2(v[2].x, v[3].x)};
      *(LAS v2u*)(tb + (16 + i16) * 168 + 40 * g + 8 * t) = (v2u){pk2(-v[0].y, -v[1].y), pk2(-v[2].y, -v[3].y)};
    }
    asm volatile("" ::: "memory");
    const unsigned ra = (8 * g + (i16 >> 2)) * 168 + (i16 & 3) * 8;
#pragma unroll
    for (int t = 0; t < 5; ++t) {
      const v2u lo = ldstr(tb, ra + 32 * t), hi = ldstr(tb, ra + 4 * 168 + 32 * t);
      __builtin_amdgcn_raw_buffer_store_b128((v4u){lo.x, lo.y, hi.x, hi.y}, rs, hst, t * 36864, 16);
    }
  }
  FENCE();
  ST(6);
  tr4(cf[1][0][0], cf[1][0][1], cf[1][0][2], cf[1][0][3], qm);
  tr4(cf[1][1][0], cf[1][1][1], cf[1][1][2], cf[1][1][3], qm);
  tr4(cf[1][2][0], cf[1][2][1], cf[1][2][2], cf[1][2][3], qm);
  tr4(cf[1][3][0], cf[1][3][1], cf[1][3][2], cf[1][3][3], qm);
  tr4(c8[0], c8[1], c8[2], c8[3], qm);
  LAS char* c8s = sm + 32768;
#pragma unroll
  for (int a = 0; a < 4; ++a) *(LAS v4u*)(c8s + (a * 4 + w) * 1024 + lane * 16) = c8[a];
  const v2u dsk = *(const GAS v2u*)(p.DskA + 64 * s + 4 * i16);
  BARRIER();
  ST(7);

  const unsigned hld = WS_H + (unsigned)(c * 180 + 9 * w) * 1024 + lane * 16;
  v4u hf[2][9];
  auto load_h = [&](int t) {
#pragma unroll
    for (int i = 0; i < 9; ++i) hf[t & 1][i] = __builtin_amdgcn_raw_buffer_load_b128(rs, hld + (i & 3) * 1024, t * 36864 + (i >> 2) * 4096, 16);
  };
  auto bad_h = [&](int t) {
    unsigned m = 0;
#pragma unroll
    for (int i = 0; i < 9; ++i) { const v4u v = hf[t & 1][i]; m = max(max(m, v.x), v.y); m = max(max(m, v.z), v.w); }
    return __any(m == ~0u);
  };
  LAS char* red = sm + 49152;
  const float dk[4] = {bf2f(dsk.x & 0xffffu), bfhi(dsk.x), bf2f(dsk.y & 0xffffu), bfhi(dsk.y)};
  load_h(0); load_h(1);
  FENCE();
#pragma unroll
  for (int t = 0; t < 5; ++t) {
#pragma unroll 1
    for (unsigned spin = 0; spin < SPIN_CAP; ++spin) {
      if (!bad_h(t)) break;
      asm volatile("" ::: "memory");
      load_h(t);
    }
    FENCE();
    if (t == 0) ST(8);
    f4 acc[4];
#pragma unroll
    for (int n = 0; n < 4; ++n) acc[n] = mfma16(lds16(c8s, (w * 4 + n) * 1024 + lane * 16), hf[t & 1][8], (f4){0, 0, 0, 0});
#pragma unroll
    for (int i = 0; i < 8; ++i)
#pragma unroll
      for (int n = 0; n < 4; ++n) acc[n] = mfma16(cf[i >> 2][n][i & 3], hf[t & 1][i], acc[n]);
    FENCE();
    if (t + 2 < 5) load_h(t + 2);
    FENCE();
    LAS char* rb = (t & 1) ? sm + 10240 : red;
#pragma unroll
    for (int n = 0; n < 4; ++n) *(LAS f4*)(rb + w * 4352 + i16 * 272 + n * 64 + g * 16) = acc[n];
    BARRIER();
    {
      const int tr = 4 * w + g, tl = 16 * t + tr;
      f4 y = *(LAS f4*)(rb + tr * 272 + i16 * 16);
#pragma unroll
      for (int q = 1; q < 4; ++q) y += *(LAS f4*)(rb + q * 4352 + tr * 272 + i16 * 16);
      const int gq = tl / 20, rem = tl - 20 * gq, rho = 4 * gq + (rem & 3);
      const v2u xv = *(LAS v2u*)(sm + (rem >> 2) * 2048 + rho * 128 + (((i16 >> 1) ^ ((rho >> 1) & 3)) * 16) + (i16 & 1) * 8);
      const unsigned s01 = pk2(bf2f(xv.x & 0xffffu) * dk[0], bfhi(xv.x) * dk[1]), s23 = pk2(bf2f(xv.y & 0xffffu) * dk[2], bfhi(xv.y) * dk[3]);
      y += (f4){bf2f(s01 & 0xffffu), bfhi(s01), bf2f(s23 & 0xffffu), bfhi(s23)};
      const int row = c * TC + tl;
      __builtin_amdgcn_raw_buffer_store_b128(__builtin_bit_cast(v4u, y), ro, (unsigned)row * 4608u + (64 * s + 4 * i16) * 4u, 0, OUT_AUX);
    }
    if (t == 0 && id == NA - NS && tid == 0)
      __hip_atomic_store((GAS unsigned*)(p.ws + WS_EPOCH_A), tagA, __ATOMIC_RELAXED, __HIP_MEMORY_SCOPE_AGENT);
    FENCE();
  }
  ST(9); STC(15);
}

DI void role_b(const Params& p, LAS char* sm, int b) {
  const int tid = threadIdx.x, lane = tid & 63;
  const int w = __builtin_amdgcn_readfirstlane(tid >> 6);
  const int grp = b >> 4, r16 = b & 15, ch = 64 * grp + 4 * r16 + w;
  const int i16 = lane & 15, g = lane >> 4;
  const auto rs = __builtin_amdgcn_make_buffer_rsrc((void*)p.ws, 0, WS_TOTAL, 0x00020000);
  const auto ro = __builtin_amdgcn_make_buffer_rsrc((void*)p.out, 0, L * 4608, 0x00020000);
  ST(0);
  constexpr unsigned WB = 13312;
  LAS char* kb = sm + w * WB;
  LAS char* ub = kb + 8448;
  LAS char* tile = sm + 4 * WB;

  const unsigned ep = ld_epoch(p.ws, WS_EPOCH_B + grp * 256);
  const unsigned rA = *(const GAS unsigned*)(p.lA + ch * NM + 2 * lane), rI = *(const GAS unsigned*)(p.Aim + ch * NM + 2 * lane);
  const unsigned rCr = *(const GAS unsigned*)(p.Cr + ch * NM + 2 * lane), rCi = *(const GAS unsigned*)(p.Ci + ch * NM + 2 * lane);
  const unsigned rdt = *(const GAS u16*)(p.ldtB + ch), rdk = *(const GAS u16*)(p.DskB + ch);
  v4u ut[4];
#pragma unroll
  for (int i = 0; i < 4; ++i) ut[i] = ldg16(p.u, (unsigned)(128 * r16 + 32 * w + 8 * i + (lane >> 3)) * 1152u + grp * 128u + (lane & 7) * 16u);
  FENCE();
  const unsigned tagA = ep + 1u, tagB = tag2(ep + 1u);
  if (lane < 30) *(LAS v4u*)(ub + lane * 16) = (v4u){0, 0, 0, 0};
#pragma unroll
  for (int i = 0; i < 4; ++i) *(LAS v4u*)(tile + (32 * w + 8 * i + (lane >> 3)) * 144 + (lane & 7) * 16) = ut[i];
  BARRIER();
  {
    const unsigned ra = (8 * g + (i16 >> 2)) * 144 + 32 * w + (i16 & 3) * 8;
    const unsigned so = WS_UT + (unsigned)((grp * 16 + 4 * w + (i16 >> 2)) * 16 + r16) * 1024 + g * 64 + (i16 & 3) * 16;
#pragma unroll
    for (int it = 0; it < 4; ++it) {
      const v2u lo = ldstr(tile, ra + it * 32 * 144), hi = ldstr(tile, ra + (it * 32 + 4) * 144);
      __builtin_amdgcn_raw_buffer_store_b128((v4u){lo.x, lo.y, hi.x, hi.y}, rs, so + it * 256, 0, 16);
    }
    asm volatile("s_waitcnt vmcnt(0)" ::: "memory");
    if (lane == 0) __builtin_amdgcn_raw_buffer_store_b64((v2u){tagA, tagB}, rs, WS_FLAG_U + (unsigned)((grp * 4 + w) * 16 + r16) * 8, 0, 16);
  }
  FENCE();
  ST(1);

  f4 kt[3];
  {
    const float dt = rbf(fexp(bf2f(rdt)));
    const unsigned ea = pk2(fexp(bf2f(rA & 0xffffu)), fexp(bfhi(rA)));
    float ar[2] = {-bf2f(ea & 0xffffu) * dt, -bfhi(ea) * dt};
    float ai[2] = {bf2f(rI & 0xffffu) * dt, bfhi(rI) * dt};
    f2 G[2] = {(f2){2.f * (bf2f(rCr & 0xffffu) * dt), 2.f * (bf2f(rCi & 0xffffu) * dt)}, (f2){2.f * (bfhi(rCr) * dt), 2.f * (bfhi(rCi) * dt)}};
    f2 z1[2], z2[2], z4[2], z8[2], z128[2];
#pragma unroll
    for (int m = 0; m < 2; ++m) {
      z1[m] = cexp(ar[m], ai[m]); z8[m] = cexp(8.f * ar[m], 8.f * ai[m]); z128[m] = cexp(128.f * ar[m], 128.f * ai[m]);
      z2[m] = cmul(z1[m], z1[m]); z4[m] = cmul(z2[m], z2[m]);
    }
    auto put = [&](int row, f2 a, f2 b2) { *(LAS v2u*)(kb + row * 528 + lane * 8) = (v2u){pk2(a.x, a.y), pk2(b2.x, b2.y)}; };
    const unsigned fr = i16 * 528 + g * 16;
    v4u rf[8];
    {
      f2 r0 = {1.f, 0.f}, r1 = {1.f, 0.f};
      const f2 c0 = {z8[0].x, -z8[0].y}, c1 = {z8[1].x, -z8[1].y};
#pragma unroll
      for (int j = 0; j < 16; ++j) { put(j, r0, r1); if (j < 15) { r0 = cmul(r0, c0); r1 = cmul(r1, c1); } }
      asm volatile("" ::: "memory");
#pragma unroll
      for (int k = 0; k < 8; ++k) rf[k] = lds16(kb, fr + k * 64);
      asm volatile("" ::: "memory");
    }
    auto tile_mma = [&](int t) {
      asm volatile("" ::: "memory");
      v4u lf[8];
#pragma unroll
      for (int k = 0; k < 8; ++k) lf[k] = lds16(kb, fr + k * 64);
      asm volatile("" ::: "memory");
      kt[t] = (f4){0, 0, 0, 0};
#pragma unroll
      for (int k = 0; k < 8; ++k) kt[t] = mfma16(lf[k], rf[k], kt[t]);
    };
#pragma unroll
    for (int a = 0; a < 2; ++a) {
      f2 v0 = G[0], v1 = G[1];
#pragma unroll
      for (int bb = 0; bb < 8; ++bb) { put(8 * a + bb, v0, v1); if (bb < 7) { v0 = cmul(v0, z1[0]); v1 = cmul(v1, z1[1]); } }
      G[0] = cmul(G[0], z128[0]); G[1] = cmul(G[1], z128[1]);
    }
    tile_mma(0);
#pragma unroll
    for (int a = 2; a < 4; ++a) {
      f2 v0 = G[0], v1 = G[1];
#pragma unroll
      for (int bb = 0; bb < 4; ++bb) { put(4 * (a - 2) + bb, v0, v1); if (bb < 3) { v0 = cmul(v0, z2[0]); v1 = cmul(v1, z2[1]); } }
      G[0] = cmul(G[0], z128[0]); G[1] = cmul(G[1], z128[1]);
    }
#pragma unroll
    for (int a = 4; a < 8; ++a) {
      put(8 + 2 * (a - 4), G[0], G[1]);
      put(9 + 2 * (a - 4), cmul(G[0], z4[0]), cmul(G[1], z4[1]));
      G[0] = cmul(G[0], z128[0]); G[1] = cmul(G[1], z128[1]);
    }
    tile_mma(1);
#pragma unroll
    for (int a = 8; a < 17; ++a) {
      put(a - 8, G[0], G[1]);
      if (a < 16) { G[0] = cmul(G[0], z128[0]); G[1] = cmul(G[1], z128[1]); }
    }
    tile_mma(2);
  }
  FENCE();
  {
    LAS u16* k0 = (LAS u16*)kb;
    {
      unsigned kv[4], pv[4];
#pragma unroll
      for (int r = 0; r < 4; ++r) { kv[r] = pk2(kt[2][r], kt[2][r]); pv[r] = __builtin_amdgcn_update_dpp(0u, kv[r], 0x111, 0xf, 0xf, true); }
      if (g < 2) {
#pragma unroll
        for (int r = 0; r < 4; ++r) {
          const int p0 = 128 * (8 + 4 * g + r) + 8 * i16;
          const unsigned last = (i16 & 1) ? ((kv[r] & 0xffffu) | (pv[r] & 0xffff0000u)) : kv[r];
          *(LAS v2u*)(k0 + 2044 - p0) = (v2u){kv[r], kv[r]};
          *(LAS v2u*)(k0 + 2048 - p0) = (v2u){kv[r], last};
        }
      }
      if (lane == 32) *(LAS v2u*)(k0) = (v2u){kv[0], kv[0]};
    }
    asm volatile("" ::: "memory");
    if (g >= 2) {
#pragma unroll
      for (int q = 0; q < 2; ++q) {
        const int p0 = 128 * (4 + 2 * (g - 2) + q) + 8 * i16;
        const float a0 = kt[1][2 * q], a1 = kt[1][2 * q + 1];
        *(LAS unsigned*)(k0 + 2042 - p0) = pk2(a1, a1);
        *(LAS v2u*)(k0 + 2044 - p0) = (v2u){pk2(a1, a0), pk2(a0, a0)};
        *(LAS unsigned*)(k0 + 2048 - p0) = pk2(a0, a0);
      }
    }
    asm volatile("" ::: "memory");
    if (g < 2) {
      const int p0 = 128 * (2 + g) + 8 * i16;
      const unsigned k3 = pk2(kt[1][3], kt[1][0]);
      k0[2041 - p0] = (u16)k3;
      *(LAS unsigned*)(k0 + 2042 - p0) = pk2(kt[1][2], kt[1][2]);
      *(LAS v2u*)(k0 + 2044 - p0) = (v2u){pk2(kt[1][2], kt[1][1]), pk2(kt[1][0], kt[1][0])};
      k0[2048 - p0] = (u16)(k3 >> 16);
    }
    asm volatile("" ::: "memory");
    {
      const int l0 = 128 * (g >> 1) + 8 * i16 + 4 * (g & 1);
      *(LAS v2u*)(k0 + 2044 - l0) = (v2u){pk2(kt[0][3], kt[0][2]), pk2(kt[0][1], kt[0][0])};
    }
    if (lane < 3) *(LAS v4u*)(k0 + 2048 + 8 * lane) = (v4u){0, 0, 0, 0};
    asm volatile("" ::: "memory");
#pragma unroll
    for (int it = 0; it < 5; ++it) {
      const int e = it * 64 + lane;
      if (e < 258) {
        const v4u a = lds16(kb, e * 16);
        const unsigned nx = *(LAS unsigned*)(kb + e * 16 + 16);
        *(LAS v4u*)(kb + 4160 + e * 16) = (v4u){__builtin_amdgcn_alignbyte(a.y, a.x, 2), __builtin_amdgcn_alignbyte(a.z, a.y, 2),
                                                __builtin_amdgcn_alignbyte(a.w, a.z, 2), __builtin_amdgcn_alignbyte(nx, a.w, 2)};
      }
    }
  }
  FENCE();
  ST(2);
  {
    const unsigned fo = WS_FLAG_U + (unsigned)((grp * 4 + (r16 >> 2)) * 16 + i16) * 8;
#pragma unroll 1
    for (unsigned spin = 0; spin < SPIN_CAP; ++spin) {
      asm volatile("" ::: "memory");
      const v2u f = __builtin_amdgcn_raw_buffer_load_b64(rs, fo, 0, 16);
      if (__all(f.x == tagA && f.y == tagB)) break;
      __builtin_amdgcn_s_sleep(2);
    }
    FENCE();
#pragma unroll
    for (int i = 0; i < 4; ++i) ut[i] = __builtin_amdgcn_raw_buffer_load_b128(rs, WS_UT + (unsigned)((grp * 16 + r16) * 16 + 4 * w + i) * 1024 + lane * 16, 0, 16);
#pragma unroll
    for (int i = 0; i < 4; ++i) *(LAS v4u*)(sm + (lane & 3) * WB + 8448 + 480 + (4 * w + i) * 256 + (lane >> 2) * 16) = ut[i];
  }
  BARRIER();
  ST(3);
  f4 acc[8];
#pragma unroll
  for (int t = 0; t < 8; ++t) acc[t] = (f4){0, 0, 0, 0};
  {
    const int ab = (i16 & 1) ? (2031 - i16 + 8 * g) * 2 : 4160 + (2030 - i16 + 8 * g) * 2;
    const int bb = 448 + 32 * i16 + 16 * g;
#pragma unroll 1
    for (int r = 0; r < 8; ++r) {
      v4u A[8], B[8];
#pragma unroll
      for (int q = 0; q < 8; ++q) {
        LAS unsigned* pa = (LAS unsigned*)(kb + ab - 64 * r - 512 * q);
        A[q] = (v4u){pa[0], pa[1], pa[2], pa[3]};
      }
#pragma unroll
      for (int e = 0; e < 8; ++e) B[e] = lds16(ub, bb - 64 * r + 512 * e);
#pragma unroll
      for (int q = 0; q < 8; ++q)
#pragma unroll
        for (int e = 0; e + q < 8; ++e) acc[e + q] = mfma16(A[q], B[e], acc[e + q]);
    }
  }
  FENCE();
  ST(4);
  {
    const float dk = bf2f(rdk);
    const unsigned so = WS_YT + (unsigned)(((grp * 16 + (i16 >> 3)) * 16 + r16) * 4 + w) * 256 + g * 64 + (i16 & 7) * 8;
#pragma unroll
    for (int t = 0; t < 8; ++t) {
      const v2u uv = *(LAS v2u*)(ub + 480 + (256 * t + 16 * i16 + 4 * g) * 2);
      const unsigned y01 = pk2(acc[t][0], acc[t][1]), y23 = pk2(acc[t][2], acc[t][3]);
      const unsigned s01 = pk2(bf2f(uv.x & 0xffffu) * dk, bfhi(uv.x) * dk), s23 = pk2(bf2f(uv.y & 0xffffu) * dk, bfhi(uv.y) * dk);
      const v2u o = {pk2(bf2f(y01 & 0xffffu) + bf2f(s01 & 0xffffu), bfhi(y01) + bfhi(s01)),
                     pk2(bf2f(y23 & 0xffffu) + bf2f(s23 & 0xffffu), bfhi(y23) + bfhi(s23))};
      __builtin_amdgcn_raw_buffer_store_b64(o, rs, so + t * (2 * 16 * 1024), 0, 16);
    }
    asm volatile("s_waitcnt vmcnt(0)" ::: "memory");
    if (lane == 0) __builtin_amdgcn_raw_buffer_store_b64((v2u){tagA, tagB}, rs, WS_FLAG_Y + (unsigned)(grp * 64 + 4 * r16 + w) * 8, 0, 16);
  }
  FENCE();
  ST(5);
  {
    const unsigned fo = WS_FLAG_Y + (unsigned)(grp * 64 + 16 * w + i16) * 8;
#pragma unroll 1
    for (unsigned spin = 0; spin < SPIN_CAP; ++spin) {
      asm volatile("" ::: "memory");
      const v2u f = __builtin_amdgcn_raw_buffer_load_b64(rs, fo, 0, 16);
      if (__all(f.x == tagA && f.y == tagB)) break;
      __builtin_amdgcn_s_sleep(2);
    }
    FENCE();
#pragma unroll
    for (int i = 0; i < 4; ++i) ut[i] = __builtin_amdgcn_raw_buffer_load_b128(rs, WS_YT + (unsigned)((grp * 16 + r16) * 16 + 4 * w + i) * 1024 + lane * 16, 0, 16);
    LAS u16* tp = (LAS u16*)(tile + (32 * (i16 & 3) + 4 * (i16 >> 2)) * 144) + 16 * w + g;
#pragma unroll
    for (int i = 0; i < 4; ++i) {
#pragma unroll
      for (int k = 0; k < 4; ++k) {
        const int row = 16 * (k >> 1) + 2 * (k & 1);
        tp[row * 72 + 4 * i] = (u16)ut[i][k];
        tp[(row + 1) * 72 + 4 * i] = (u16)(ut[i][k] >> 16);
      }
    }
  }
  BARRIER();
  if (r16 == 0 && tid == 0)
    __hip_atomic_store((GAS unsigned*)(p.ws + WS_EPOCH_B + grp * 256), tagA, __ATOMIC_RELAXED, __HIP_MEMORY_SCOPE_AGENT);
#pragma unroll
  for (int i = 0; i < 8; ++i) {
    const int row = 32 * w + 4 * i + g;
    const v2u v = *(LAS v2u*)(tile + row * 144 + i16 * 8);
    __builtin_amdgcn_raw_buffer_store_b128((v4u){v.x << 16, v.x & 0xffff0000u, v.y << 16, v.y & 0xffff0000u}, ro, (unsigned)(128 * r16 + row) * 4608u + (576 + 64 * grp + 4 * i16) * 4u, 0, OUT_AUX);
  }
  ST(6);
}

__global__ void __launch_bounds__(256, 2) ssm_fused(Params p) {
  extern __shared__ __attribute__((aligned(16))) char smem[];
  LAS char* sm = (LAS char*)smem;
  const int bid = blockIdx.x;
  unsigned long long pc;
  asm volatile("s_getpc_b64 %0" : "=s"(pc));
  pc &= ~255ull;
  const unsigned part = (((unsigned)bid >> 3) * 4 + (threadIdx.x >> 6)) % 5u;
  unsigned off = part * 8192u + (threadIdx.x & 63) * 128u;
  off = off < CODE_BYTES ? off : 0;
  const unsigned pf = *(const GAS unsigned*)((const GAS char*)pc + off);
  if (bid < NA) role_a(p, sm, bid);
  else role_b(p, sm, bid - NA);
  asm volatile("" :: "v"(pf));
}

extern "C" void kernel_launch(void* const* d_in, const int* in_sizes, int n_in,
                              void* d_out, int out_size, void* d_ws, size_t ws_size,
                              hipStream_t stream) {
  Params p;
  p.x = (const u16*)d_in[0]; p.Lre = (const u16*)d_in[1]; p.Lim = (const u16*)d_in[2];
  p.Bre = (const u16*)d_in[3]; p.Bim = (const u16*)d_in[4]; p.Cre = (const u16*)d_in[5]; p.Cim = (const u16*)d_in[6];
  p.ldtA = (const u16*)d_in[7]; p.DskA = (const u16*)d_in[8];
  p.u = (const u16*)d_in[9]; p.lA = (const u16*)d_in[10]; p.Aim = (const u16*)d_in[11];
  p.Cr = (const u16*)d_in[12]; p.Ci = (const u16*)d_in[13]; p.ldtB = (const u16*)d_in[14]; p.DskB = (const u16*)d_in[15];
  p.out = (float*)d_out; p.ws = (char*)d_ws;
  (void)hipFuncSetAttribute((const void*)ssm_fused, hipFuncAttributeMaxDynamicSharedMemorySize, LDS_BYTES);
  ssm_fused<<<NA + GRID_B, 256, LDS_BYTES, stream>>>(p);
}
```

```cpp
#define PROBE 99
#define PROBE_BID 0
#define OUT_AUX 17
#include <hip/hip_runtime.h>
#include <hip/hip_bf16.h>
#include <stdint.h>

typedef unsigned short u16;
typedef unsigned v4u __attribute__((ext_vector_type(4)));
typedef unsigned v2u __attribute__((ext_vector_type(2)));
typedef float f2 __attribute__((ext_vector_type(2)));
typedef float f4 __attribute__((ext_vector_type(4)));
typedef __bf16 bf2 __attribute__((ext_vector_type(2)));
typedef __bf16 bf8 __attribute__((ext_vector_type(8)));
typedef short s4 __attribute__((ext_vector_type(4)));
#define GAS __attribute__((address_space(1)))
#define LAS __attribute__((address_space(3)))
#define DI __device__ __forceinline__

constexpr int L = 2048, D = 576, NM = 128;
constexpr int TC = 80, NCH = 26, NS = 9;
constexpr int NA = NCH * NS;
constexpr int NB = 144;
constexpr int LDS_BYTES = 81920;

constexpr unsigned WS_EPOCH_A = 0;
constexpr unsigned WS_EPOCH_B = 256;
constexpr unsigned WS_FLAG_U = 4096;
constexpr unsigned WS_FLAG_Y = 4096 + 8192;
constexpr unsigned WS_E = 32768;
constexpr unsigned WS_E_BYTES = NCH * 36 * 256;
constexpr unsigned WS_H = WS_E + 262144;
constexpr unsigned WS_H_BYTES = NCH * 5 * 36 * 1024;
constexpr unsigned WS_UT = WS_H + WS_H_BYTES;
constexpr unsigned WS_UT_BYTES = 9 * 16 * 16 * 1024;
constexpr unsigned WS_YT = WS_UT + WS_UT_BYTES;
constexpr unsigned WS_YT_BYTES = 9 * 16 * 16 * 1024;
constexpr unsigned WS_TOTAL = WS_YT + WS_YT_BYTES;
#define ST(i) do { if ((i) == PROBE) tpp = __builtin_amdgcn_s_memrealtime(); } while (0)
#define STC(i) do {} while (0)

#define NPOISON 5
#define GRID_B NB
constexpr unsigned CODE_BYTES = 39000;
constexpr unsigned SPIN_CAP = 1u << 17;

struct Params {
  const u16 *x, *Lre, *Lim, *Bre, *Bim, *Cre, *Cim, *ldtA, *DskA;
  const u16 *u, *lA, *Aim, *Cr, *Ci, *ldtB, *DskB;
  float* out;
  char* ws;
};

DI float bf2f(unsigned v) { return __uint_as_float(v << 16); }
DI float bfhi(unsigned v) { return __uint_as_float(v & 0xffff0000u); }
DI unsigned pk2(float a, float b) { f2 z = {a, b}; return __builtin_bit_cast(unsigned, __builtin_convertvector(z, bf2)); }
DI float rbf(float f) { return bf2f(pk2(f, 0.f) & 0xffffu); }
DI f2 cmul(f2 a, f2 b) {
  f2 t = a * (f2){b.x, b.x};
  return __builtin_elementwise_fma((f2){a.y, a.x}, (f2){-b.y, b.y}, t);
}
DI f2 cfma(f2 a, f2 b, f2 c) {
  f2 t = __builtin_elementwise_fma(a, (f2){b.x, b.x}, c);
  return __builtin_elementwise_fma((f2){a.y, a.x}, (f2){-b.y, b.y}, t);
}
DI float fexp(float x) {
  const float c_hi = 1.44269502162933349609375f, c_lo = 1.925963033500011e-8f;
  float t = x * c_hi;
  float e = __builtin_fmaf(x, c_hi, -t);
  e = __builtin_fmaf(x, c_lo, e);
  float r = __builtin_amdgcn_exp2f(t);
  return __builtin_fmaf(r * e, 0.693147180559945f, r);
}
DI f2 cexp(float re, float im) {
  const float r_hi = 0.15915494f, r_lo = 6.4206382e-9f;
  float m = fexp(re);
  float t = im * r_hi;
  float e = __builtin_fmaf(im, r_hi, -t);
  e = __builtin_fmaf(im, r_lo, e);
  float f = __builtin_amdgcn_fractf(t) + e;
  return (f2){m * __builtin_amdgcn_cosf(f), m * __builtin_amdgcn_sinf(f)};
}
DI f4 mfma16(v4u a, v4u b, f4 c) {
  return __builtin_amdgcn_mfma_f32_16x16x32_bf16(__builtin_bit_cast(bf8, a), __builtin_bit_cast(bf8, b), c, 0, 0, 0);
}
DI v4u ldg16(const void* base, unsigned off, int cst = 0) { return *(const GAS v4u*)((const GAS char*)base + off + cst); }
DI v4u lds16(LAS char* s, unsigned off) { return *(LAS v4u*)(s + off); }
DI v2u ldstr(LAS char* s, unsigned off) {
  return __builtin_bit_cast(v2u, __builtin_amdgcn_ds_read_tr16_b64_v4i16((LAS s4*)(s + off)));
}
#define FENCE() do { __builtin_amdgcn_sched_barrier(0); asm volatile("" ::: "memory"); } while (0)
#define BARRIER() do { __builtin_amdgcn_sched_barrier(0); asm volatile("s_waitcnt lgkmcnt(0)\n\ts_barrier" ::: "memory"); __builtin_amdgcn_sched_barrier(0); } while (0)

struct QMask { unsigned long long e1, o1, e2, o2; };
#define TRC(d, s0, s1, P) "v_cndmask_b32_dpp " d ", " s0 ", " s1 ", vcc quad_perm:" P " row_mask:0xf bank_mask:0xf\n\t"
DI void tr4half(v2u& a0, v2u& a1, v2u& a2, v2u& a3, const QMask& m) {
  unsigned a00 = a0.x, a01 = a0.y, a10 = a1.x, a11 = a1.y, a20 = a2.x, a21 = a2.y, a30 = a3.x, a31 = a3.y;
  unsigned t00, t01, t20, t21;
  asm volatile(
      "s_mov_b64 vcc, %12\n\ts_nop 0\n\t"
      TRC("%8", "%2", "%0", "[1,0,3,2]") TRC("%9", "%3", "%1", "[1,0,3,2]")
      TRC("%10", "%6", "%4", "[1,0,3,2]") TRC("%11", "%7", "%5", "[1,0,3,2]")
      "s_mov_b64 vcc, %13\n\t"
      TRC("%2", "%0", "%2", "[1,0,3,2]") TRC("%3", "%1", "%3", "[1,0,3,2]")
      TRC("%6", "%4", "%6", "[1,0,3,2]") TRC("%7", "%5", "%7", "[1,0,3,2]")
      "s_mov_b64 vcc, %14\n\t"
      TRC("%0", "%10", "%8", "[2,3,0,1]") TRC("%1", "%11", "%9", "[2,3,0,1]")
      TRC("%4", "%6", "%2", "[2,3,0,1]") TRC("%5", "%7", "%3", "[2,3,0,1]")
      "s_mov_b64 vcc, %15\n\t"
      TRC("%10", "%8", "%10", "[2,3,0,1]") TRC("%11", "%9", "%11", "[2,3,0,1]")
      TRC("%6", "%2", "%6", "[2,3,0,1]") TRC("%7", "%3", "%7", "[2,3,0,1]")
      "s_nop 0"
      : "+v"(a00), "+v"(a01), "+v"(a10), "+v"(a11), "+v"(a20), "+v"(a21), "+v"(a30), "+v"(a31),
        "=&v"(t00), "=&v"(t01), "=&v"(t20), "=&v"(t21)
      : "s"(m.e1), "s"(m.o1), "s"(m.e2), "s"(m.o2)
      : "vcc");
  a0 = (v2u){a00, a01}; a1 = (v2u){a20, a21}; a2 = (v2u){t20, t21}; a3 = (v2u){a30, a31};
}
DI void tr4(v4u& a0, v4u& a1, v4u& a2, v4u& a3, const QMask& m) {
  v2u l0 = a0.xy, l1 = a1.xy, l2 = a2.xy, l3 = a3.xy, h0 = a0.zw, h1 = a1.zw, h2 = a2.zw, h3 = a3.zw;
  tr4half(l0, l1, l2, l3, m);
  tr4half(h0, h1, h2, h3, m);
  a0 = (v4u){l0.x, l0.y, h0.x, h0.y}; a1 = (v4u){l1.x, l1.y, h1.x, h1.y};
  a2 = (v4u){l2.x, l2.y, h2.x, h2.y}; a3 = (v4u){l3.x, l3.y, h3.x, h3.y};
}
DI float xsum_groups(float v) {
  auto a = __builtin_amdgcn_permlane16_swap(__float_as_uint(v), __float_as_uint(v), false, false);
  v = __uint_as_float(a[0]) + __uint_as_float(a[1]);
  auto b = __builtin_amdgcn_permlane32_swap(__float_as_uint(v), __float_as_uint(v), false, false);
  return __uint_as_float(b[0]) + __uint_as_float(b[1]);
}
DI unsigned tag2(unsigned t) { return (t * 0x9E3779B1u) ^ 0x5bd1e995u; }
DI unsigned ld_epoch(const char* ws, unsigned off) {
  return __hip_atomic_load((const GAS unsigned*)(ws + off), __ATOMIC_RELAXED, __HIP_MEMORY_SCOPE_AGENT);
}

DI void role_a(const Params& p, LAS char* sm, int bid, unsigned long long& tpp) {
  const int tid = threadIdx.x, lane = tid & 63;
  const int w = __builtin_amdgcn_readfirstlane(tid >> 6);
  int id; { const int q = NA / 8, r = NA % 8, xcd = bid & 7, off = bid >> 3; id = (xcd < r ? xcd * (q + 1) : r * (q + 1) + (xcd - r) * q) + off; }
  const int c = id / NS, s = id % NS;
  const int i16 = lane & 15, g = lane >> 4, c0 = lane & 3, c1 = (lane >> 2) & 3;
  const QMask qm = {0x5555555555555555ull, 0xAAAAAAAAAAAAAAAAull, 0x3333333333333333ull, 0xCCCCCCCCCCCCCCCCull};
  const auto rs = __builtin_amdgcn_make_buffer_rsrc((void*)p.ws, 0, WS_TOTAL, 0x00020000);
  const auto ro = __builtin_amdgcn_make_buffer_rsrc((void*)p.out, 0, L * 4608, 0x00020000);
  __builtin_amdgcn_s_setprio(2);
  ST(0); STC(14);

  const unsigned ep = ld_epoch(p.ws, WS_EPOCH_A);
  const int pl = 64 * s + 8 * w + (i16 & 7) + 32 * (i16 >> 3);
  const unsigned rLre = *(const GAS u16*)(p.Lre + pl), rLim = *(const GAS u16*)(p.Lim + pl), rLdt = *(const GAS u16*)(p.ldtA + pl);
  const int kap = 4 * s + w;
  const unsigned hslot = kap < 32 ? 9 * (kap >> 3) + (kap & 7) : 9 * (kap - 32) + 8;
  const unsigned hst = WS_H + (unsigned)(c * 180 + hslot) * 1024 + lane * 16;
  {
    const v4u pz = {~0u, ~0u, ~0u, ~0u};
#pragma unroll
    for (int t = 0; t < NPOISON; ++t) __builtin_amdgcn_raw_buffer_store_b128(pz, rs, hst, t * 36864, 16);
  }
  FENCE();
  auto kb_of = [&](int ord) { int k = s + 1 + ord; return k >= 9 ? k - 9 : k; };
  const auto rx = __builtin_amdgcn_make_buffer_rsrc((void*)p.x, 0, L * D * 2, 0x00020000);
  const auto rbr = __builtin_amdgcn_make_buffer_rsrc((void*)p.Bre, 0, D * D * 2, 0x00020000);
  const auto rbi = __builtin_amdgcn_make_buffer_rsrc((void*)p.Bim, 0, D * D * 2, 0x00020000);
  const unsigned xv0 = (unsigned)(20 * (lane >> 5) + ((lane >> 3) & 3)) * 1152u + (lane & 7) * 16u;
  auto ldx = [&](int ord, int sub) {
    return __builtin_amdgcn_raw_buffer_load_b128(rx, xv0, (c * TC + 40 * (sub & 1) + 4 * (sub >> 1)) * 1152 + kb_of(ord) * 128, 0);
  };
  const unsigned xw = (lane >> 3) * 128 + (((lane & 7) ^ (lane >> 4)) * 16);
  LAS char* xwb = sm + xw + (w >> 1) * 10240 + (w & 1) * 5120;
  const unsigned brow = (unsigned)(4 * (c1 & 1) + 32 * (c1 >> 1)) * 1152u;
  const int bso = (64 * s + 8 * w) * 1152;

  v4u xs[5][5], br[4][4], bi[4][4], bm[4];
  auto load_x = [&](int G) {
#pragma unroll
    for (int i = 0; i < 5; ++i) xs[G][i] = ldx(2 * G + (w >> 1), 5 * (w & 1) + i);
  };
  auto load_b = [&](int G) {
    const unsigned o = brow + (unsigned)(g >> 1 ? kb_of(2 * G + 1) : kb_of(2 * G)) * 128u + (4 * (g & 1) + c0) * 16u;
#pragma unroll
    for (int a = 0; a < 4; ++a) br[G][a] = __builtin_amdgcn_raw_buffer_load_b128(rbr, o + a * 1152, bso, 0);
#pragma unroll
    for (int a = 0; a < 4; ++a) bi[G][a] = __builtin_amdgcn_raw_buffer_load_b128(rbi, o + a * 1152, bso, 0);
  };
  auto store_x = [&](int G) {
#pragma unroll
    for (int i = 0; i < 5; ++i) *(LAS v4u*)(xwb + G * 20480 + i * 1024) = xs[G][i];
  };
  load_x(0); load_b(0); load_x(1); load_b(1); load_x(2); load_b(2);
  FENCE();
  ST(10);

  const float dt = rbf(fexp(bf2f(rLdt)));
  const float lre = bf2f(rLre), lim = bf2f(rLim);
  const float ar = dt * lre, ai = dt * lim;
  const f2 lam = cexp(ar, ai);
  f2 coef2;
  { const float nr = lam.x - 1.0f, ni = lam.y, den = 2.0f / (lre * lre + lim * lim);
    coef2 = (f2){(nr * lre + ni * lim) * den, (ni * lre - nr * lim) * den}; }
  const float fe = (float)(20 * (3 - g));
  const f2 wend = cexp(ar * fe, ai * fe);
  const unsigned tagA = ep + 1u, tagB = tag2(ep + 1u);
  FENCE();
  ST(11);

  f4 aR[5], aI[5];
#pragma unroll
  for (int t = 0; t < 5; ++t) { aR[t] = (f4){0, 0, 0, 0}; aI[t] = (f4){0, 0, 0, 0}; }
  const unsigned fsw = (i16 >> 1) & 3;
  const unsigned ard = (g >> 1) * 10240 + i16 * 128 + (g & 1) * 64;
  auto mma_group = [&](int G) {
    tr4(br[G][0], br[G][1], br[G][2], br[G][3], qm);
    tr4(bi[G][0], bi[G][1], bi[G][2], bi[G][3], qm);
#pragma unroll
    for (int k = 0; k < 4; ++k) {
      v4u xa[5];
#pragma unroll
      for (int t = 0; t < 5; ++t) xa[t] = lds16(sm, G * 20480 + t * 2048 + ard + ((k ^ fsw) * 16));
#pragma unroll
      for (int t = 0; t < 5; ++t) { aR[t] = mfma16(xa[t], br[G][k], aR[t]); aI[t] = mfma16(xa[t], bi[G][k], aI[t]); }
    }
  };
  v4u cf[2][4][4], c8[4];
  const char* cbase = (const char*)(g >> 1 ? p.Cim : p.Cre) + (unsigned)(64 * s + 4 * c1) * 1152u + (32 * (g & 1) + 8 * c0) * 2u;
  auto load_c = [&](int b, int n) {
#pragma unroll
    for (int a = 0; a < 4; ++a) cf[b][n][a] = ldg16(cbase, (unsigned)(16 * n * 1152 + 2 * w * 128), a * 1152 + b * 128);
  };

  store_x(0);
  BARRIER();
  ST(1);
  mma_group(0);
  FENCE();
  ST(12);
  load_x(3);
  load_b(3);
  FENCE();
  store_x(1); BARRIER();
  mma_group(1);
  FENCE();
#pragma unroll
  for (int i = 0; i < 3; ++i) { int sub = w + 4 * i; sub = sub < 10 ? sub : 9; xs[4][i] = ldx(8, sub); }
  {
    const char* mb = (const char*)(c0 >> 1 ? p.Bim : p.Bre);
    const unsigned o = bso + brow + (unsigned)kb_of(8) * 128u + (4 * (g & 1) + 2 * (g >> 1) + (c0 & 1)) * 16u;
#pragma unroll
    for (int a = 0; a < 4; ++a) bm[a] = ldg16(mb, o, a * 1152);
  }
  load_c(0, 0);
  FENCE();
  store_x(2);
  BARRIER();
  mma_group(2);
  FENCE();
  load_c(0, 1); load_c(0, 2);
  FENCE();
  store_x(3);
#pragma unroll
  for (int i = 0; i < 3; ++i) { int sub = w + 4 * i; sub = sub < 10 ? sub : 9; *(LAS v4u*)(sm + sub * 1024 + xw) = xs[4][i]; }
  BARRIER();
  mma_group(3);
  FENCE();
  load_c(0, 3);
  FENCE();
  {
    tr4(bm[0], bm[1], bm[2], bm[3], qm);
    const unsigned a4 = i16 * 128;
#pragma unroll
    for (int k = 0; k < 2; ++k) {
      v4u xa[5];
#pragma unroll
      for (int t = 0; t < 5; ++t) xa[t] = lds16(sm, t * 2048 + a4 + (((4 * (g & 1) + 2 * (g >> 1) + k) ^ fsw) * 16));
#pragma unroll
      for (int t = 0; t < 5; ++t) { aR[t] = mfma16(xa[t], bm[k], aR[t]); aI[t] = mfma16(xa[t], bm[2 + k], aI[t]); }
    }
  }
  FENCE();
  ST(2);

  f2 h[20];
  h[0] = (f2){aR[0][0], aI[0][0]};
#pragma unroll
  for (int j = 1; j < 20; ++j) h[j] = cfma(h[j - 1], lam, (f2){aR[j >> 2][j & 3], aI[j >> 2][j & 3]});
  {
    const f2 e = cmul(h[19], wend);
    const float er = xsum_groups(e.x), ei = xsum_groups(e.y);
    const v4u gr = {__float_as_uint(er), tagA, __float_as_uint(ei), tagB};
    if (g == 0) __builtin_amdgcn_raw_buffer_store_b128(gr, rs, WS_E + (unsigned)(c * 36 + kap) * 256 + i16 * 16, 0, 16);
  }
  FENCE();
  ST(3);
  f2 pre;
  {
    const f2 l20 = cexp(ar * 20.f, ai * 20.f);
    auto r16 = __builtin_amdgcn_permlane16_swap(__float_as_uint(h[19].x), __float_as_uint(h[19].x), false, false);
    auto i16s = __builtin_amdgcn_permlane16_swap(__float_as_uint(h[19].y), __float_as_uint(h[19].y), false, false);
    auto r02 = __builtin_amdgcn_permlane32_swap(r16[0], r16[0], false, false);
    auto i02 = __builtin_amdgcn_permlane32_swap(i16s[0], i16s[0], false, false);
    auto r13 = __builtin_amdgcn_permlane32_swap(r16[1], r16[1], false, false);
    auto i13 = __builtin_amdgcn_permlane32_swap(i16s[1], i16s[1], false, false);
    const f2 e0 = {__uint_as_float(r02[0]), __uint_as_float(i02[0])}, e2 = {__uint_as_float(r02[1]), __uint_as_float(i02[1])};
    const f2 e1 = {__uint_as_float(r13[0]), __uint_as_float(i13[0])};
    const f2 p2 = cfma(e0, l20, e1), p3 = cfma(p2, l20, e2);
    pre = g == 0 ? (f2){0.f, 0.f} : g == 1 ? e0 : g == 2 ? p2 : p3;
  }
  f2 lt[20];
  lt[0] = cmul(coef2, lam);
#pragma unroll
  for (int j = 1; j < 20; ++j) lt[j] = cmul(lt[j - 1], lam);
#pragma unroll
  for (int j = 0; j < 20; ++j) h[j] = cmul(h[j], coef2);
  const float f0 = (float)(80 * ((c - 1 - g) & 3));
  const f2 wq = cexp(ar * f0, ai * f0), w320 = cexp(ar * 320.f, ai * 320.f);
  const float fg = (float)(20 * g);
  const f2 wg20 = cexp(ar * fg, ai * fg);
  tr4(cf[0][0][0], cf[0][0][1], cf[0][0][2], cf[0][0][3], qm);
  tr4(cf[0][1][0], cf[0][1][1], cf[0][1][2], cf[0][1][3], qm);
  tr4(cf[0][2][0], cf[0][2][1], cf[0][2][2], cf[0][2][3], qm);
  tr4(cf[0][3][0], cf[0][3][1], cf[0][3][2], cf[0][3][3], qm);
  FENCE();
  ST(4);

  f2 H = {0.f, 0.f};
  {
    const int kq = lane < 8 ? 8 * w + lane : 32 + w;
    const unsigned oq = WS_E + (unsigned)(c * 36 + kq) * 256;
    const unsigned oe = WS_E + (unsigned)(g * 36 + kap) * 256 + i16 * 16;
    v4u e[7];
#pragma unroll 1
    for (unsigned spin = 0; spin < SPIN_CAP; ++spin) {
      asm volatile("" ::: "memory");
      const v4u q = __builtin_amdgcn_raw_buffer_load_b128(rs, oq, 0, 16);
#pragma unroll
      for (int m = 0; m < 7; ++m) e[m] = __builtin_amdgcn_raw_buffer_load_b128(rs, g + 4 * m < c ? oe + m * (4 * 36 * 256) : oq, 0, 16);
      bool ok = q.y == tagA && q.w == tagB;
#pragma unroll
      for (int m = 0; m < 7; ++m) ok = ok && e[m].y == tagA && e[m].w == tagB;
      if (__all(ok)) break;
      __builtin_amdgcn_s_sleep(1);
    }
#pragma unroll
    for (int m = 0; m < 7; ++m) {
      const f2 nx = cfma(H, w320, (f2){__uint_as_float(e[m].x), __uint_as_float(e[m].z)});
      H = g + 4 * m < c ? nx : H;
    }
    H = cmul(H, wq);
    H = (f2){xsum_groups(H.x), xsum_groups(H.y)};
  }
  FENCE();
  ST(5);
  load_c(1, 0); load_c(1, 1); load_c(1, 2); load_c(1, 3);
#pragma unroll
  for (int a = 0; a < 4; ++a) c8[a] = ldg16(cbase, (unsigned)(16 * w) * 1152u, a * 1152 + 8 * 128);
  FENCE();
  {
    const f2 car = cfma(H, wg20, pre);
    LAS char* tb = sm + 10240 + w * 5376;
#pragma unroll
    for (int t = 0; t < 5; ++t) {
      f2 v[4];
#pragma unroll
      for (int r = 0; r < 4; ++r) v[r] = cfma(lt[4 * t + r], car, h[4 * t + r]);
      *(LAS v2u*)(tb + i16 * 168 + 40 * g + 8 * t) = (v2u){pk2(v[0].x, v[1].x), pk2(v[2].x, v[3].x)};
      *(LAS v2u*)(tb + (16 + i16) * 168 + 40 * g + 8 * t) = (v2u){pk2(-v[0].y, -v[1].y), pk2(-v[2].y, -v[3].y)};
    }
    asm volatile("" ::: "memory");
    const unsigned ra = (8 * g + (i16 >> 2)) * 168 + (i16 & 3) * 8;
#pragma unroll
    for (int t = 0; t < 5; ++t) {
      const v2u lo = ldstr(tb, ra + 32 * t), hi = ldstr(tb, ra + 4 * 168 + 32 * t);
      __builtin_amdgcn_raw_buffer_store_b128((v4u){lo.x, lo.y, hi.x, hi.y}, rs, hst, t * 36864, 16);
    }
  }
  FENCE();
  ST(6);
  tr4(cf[1][0][0], cf[1][0][1], cf[1][0][2], cf[1][0][3], qm);
  tr4(cf[1][1][0], cf[1][1][1], cf[1][1][2], cf[1][1][3], qm);
  tr4(cf[1][2][0], cf[1][2][1], cf[1][2][2], cf[1][2][3], qm);
  tr4(cf[1][3][0], cf[1][3][1], cf[1][3][2], cf[1][3][3], qm);
  tr4(c8[0], c8[1], c8[2], c8[3], qm);
  LAS char* c8s = sm + 32768;
#pragma unroll
  for (int a = 0; a < 4; ++a) *(LAS v4u*)(c8s + (a * 4 + w) * 1024 + lane * 16) = c8[a];
  const v2u dsk = *(const GAS v2u*)(p.DskA + 64 * s + 4 * i16);
  BARRIER();
  ST(7);

  const unsigned hld = WS_H + (unsigned)(c * 180 + 9 * w) * 1024 + lane * 16;
  v4u hf[2][9];
  auto load_h = [&](int t) {
#pragma unroll
    for (int i = 0; i < 9; ++i) hf[t & 1][i] = __builtin_amdgcn_raw_buffer_load_b128(rs, hld + (i & 3) * 1024, t * 36864 + (i >> 2) * 4096, 16);
  };
  auto bad_h = [&](int t) {
    unsigned m = 0;
#pragma unroll
    for (int i = 0; i < 9; ++i) { const v4u v = hf[t & 1][i]; m = max(max(m, v.x), v.y); m = max(max(m, v.z), v.w); }
    return __any(m == ~0u);
  };
  LAS char* red = sm + 49152;
  const float dk[4] = {bf2f(dsk.x & 0xffffu), bfhi(dsk.x), bf2f(dsk.y & 0xffffu), bfhi(dsk.y)};
  load_h(0); load_h(1);
  FENCE();
#pragma unroll
  for (int t = 0; t < 5; ++t) {
#pragma unroll 1
    for (unsigned spin = 0; spin < SPIN_CAP; ++spin) {
      if (!bad_h(t)) break;
      asm volatile("" ::: "memory");
      load_h(t);
    }
    FENCE();
    if (t == 0) ST(8);
    f4 acc[4];
#pragma unroll
    for (int n = 0; n < 4; ++n) acc[n] = mfma16(lds16(c8s, (w * 4 + n) * 1024 + lane * 16), hf[t & 1][8], (f4){0, 0, 0, 0});
#pragma unroll
    for (int i = 0; i < 8; ++i)
#pragma unroll
      for (int n = 0; n < 4; ++n) acc[n] = mfma16(cf[i >> 2][n][i & 3], hf[t & 1][i], acc[n]);
    FENCE();
    if (t + 2 < 5) load_h(t + 2);
    FENCE();
    LAS char* rb = (t & 1) ? sm + 10240 : red;
#pragma unroll
    for (int n = 0; n < 4; ++n) *(LAS f4*)(rb + w * 4352 + i16 * 272 + n * 64 + g * 16) = acc[n];
    BARRIER();
    {
      const int tr = 4 * w + g, tl = 16 * t + tr;
      f4 y = *(LAS f4*)(rb + tr * 272 + i16 * 16);
#pragma unroll
      for (int q = 1; q < 4; ++q) y += *(LAS f4*)(rb + q * 4352 + tr * 272 + i16 * 16);
      const int gq = tl / 20, rem = tl - 20 * gq, rho = 4 * gq + (rem & 3);
      const v2u xv = *(LAS v2u*)(sm + (rem >> 2) * 2048 + rho * 128 + (((i16 >> 1) ^ ((rho >> 1) & 3)) * 16) + (i16 & 1) * 8);
      const unsigned s01 = pk2(bf2f(xv.x & 0xffffu) * dk[0], bfhi(xv.x) * dk[1]), s23 = pk2(bf2f(xv.y & 0xffffu) * dk[2], bfhi(xv.y) * dk[3]);
      y += (f4){bf2f(s01 & 0xffffu), bfhi(s01), bf2f(s23 & 0xffffu), bfhi(s23)};
      const int row = c * TC + tl;
      __builtin_amdgcn_raw_buffer_store_b128(__builtin_bit_cast(v4u, y), ro, (unsigned)row * 4608u + (64 * s + 4 * i16) * 4u, 0, OUT_AUX);
    }
    if (t == 0 && id == NA - NS && tid == 0)
      __hip_atomic_store((GAS unsigned*)(p.ws + WS_EPOCH_A), tagA, __ATOMIC_RELAXED, __HIP_MEMORY_SCOPE_AGENT);
    FENCE();
  }
  ST(9); STC(15);

}

DI void role_b(const Params& p, LAS char* sm, int b, unsigned long long& tpp) {
  const int tid = threadIdx.x, lane = tid & 63;
  const int w = __builtin_amdgcn_readfirstlane(tid >> 6);
  const int grp = b >> 4, r16 = b & 15, ch = 64 * grp + 4 * r16 + w;
  const int i16 = lane & 15, g = lane >> 4;
  const auto rs = __builtin_amdgcn_make_buffer_rsrc((void*)p.ws, 0, WS_TOTAL, 0x00020000);
  const auto ro = __builtin_amdgcn_make_buffer_rsrc((void*)p.out, 0, L * 4608, 0x00020000);
  ST(0);
  constexpr unsigned WB = 13312;
  LAS char* kb = sm + w * WB;
  LAS char* ub = kb + 8448;
  LAS char* tile = sm + 4 * WB;

  const unsigned ep = ld_epoch(p.ws, WS_EPOCH_B + grp * 256);
  const unsigned rA = *(const GAS unsigned*)(p.lA + ch * NM + 2 * lane), rI = *(const GAS unsigned*)(p.Aim + ch * NM + 2 * lane);
  const unsigned rCr = *(const GAS unsigned*)(p.Cr + ch * NM + 2 * lane), rCi = *(const GAS unsigned*)(p.Ci + ch * NM + 2 * lane);
  const unsigned rdt = *(const GAS u16*)(p.ldtB + ch), rdk = *(const GAS u16*)(p.DskB + ch);
  v4u ut[4];
#pragma unroll
  for (int i = 0; i < 4; ++i) ut[i] = ldg16(p.u, (unsigned)(128 * r16 + 32 * w + 8 * i + (lane >> 3)) * 1152u + grp * 128u + (lane & 7) * 16u);
  FENCE();
  const unsigned tagA = ep + 1u, tagB = tag2(ep + 1u);
  if (lane < 30) *(LAS v4u*)(ub + lane * 16) = (v4u){0, 0, 0, 0};
#pragma unroll
  for (int i = 0; i < 4; ++i) *(LAS v4u*)(tile + (32 * w + 8 * i + (lane >> 3)) * 144 + (lane & 7) * 16) = ut[i];
  BARRIER();
  {
    const unsigned ra = (8 * g + (i16 >> 2)) * 144 + 32 * w + (i16 & 3) * 8;
    const unsigned so = WS_UT + (unsigned)((grp * 16 + 4 * w + (i16 >> 2)) * 16 + r16) * 1024 + g * 64 + (i16 & 3) * 16;
#pragma unroll
    for (int it = 0; it < 4; ++it) {
      const v2u lo = ldstr(tile, ra + it * 32 * 144), hi = ldstr(tile, ra + (it * 32 + 4) * 144);
      __builtin_amdgcn_raw_buffer_store_b128((v4u){lo.x, lo.y, hi.x, hi.y}, rs, so + it * 256, 0, 16);
    }
    asm volatile("s_waitcnt vmcnt(0)" ::: "memory");
    if (lane == 0) __builtin_amdgcn_raw_buffer_store_b64((v2u){tagA, tagB}, rs, WS_FLAG_U + (unsigned)((grp * 4 + w) * 16 + r16) * 8, 0, 16);
  }
  FENCE();
  ST(1);

  f4 kt[3];
  {
    const float dt = rbf(fexp(bf2f(rdt)));
    const unsigned ea = pk2(fexp(bf2f(rA & 0xffffu)), fexp(bfhi(rA)));
    float ar[2] = {-bf2f(ea & 0xffffu) * dt, -bfhi(ea) * dt};
    float ai[2] = {bf2f(rI & 0xffffu) * dt, bfhi(rI) * dt};
    f2 G[2] = {(f2){2.f * (bf2f(rCr & 0xffffu) * dt), 2.f * (bf2f(rCi & 0xffffu) * dt)}, (f2){2.f * (bfhi(rCr) * dt), 2.f * (bfhi(rCi) * dt)}};
    f2 z1[2], z2[2], z4[2], z8[2], z128[2];
#pragma unroll
    for (int m = 0; m < 2; ++m) {
      z1[m] = cexp(ar[m], ai[m]); z8[m] = cexp(8.f * ar[m], 8.f * ai[m]); z128[m] = cexp(128.f * ar[m], 128.f * ai[m]);
      z2[m] = cmul(z1[m], z1[m]); z4[m] = cmul(z2[m], z2[m]);
    }
    auto put = [&](int row, f2 a, f2 b2) { *(LAS v2u*)(kb + row * 528 + lane * 8) = (v2u){pk2(a.x, a.y), pk2(b2.x, b2.y)}; };
    const unsigned fr = i16 * 528 + g * 16;
    v4u rf[8];
    {
      f2 r0 = {1.f, 0.f}, r1 = {1.f, 0.f};
      const f2 c0 = {z8[0].x, -z8[0].y}, c1 = {z8[1].x, -z8[1].y};
#pragma unroll
      for (int j = 0; j < 16; ++j) { put(j, r0, r1); if (j < 15) { r0 = cmul(r0, c0); r1 = cmul(r1, c1); } }
      asm volatile("" ::: "memory");
#pragma unroll
      for (int k = 0; k < 8; ++k) rf[k] = lds16(kb, fr + k * 64);
      asm volatile("" ::: "memory");
    }
    auto tile_mma = [&](int t) {
      asm volatile("" ::: "memory");
      v4u lf[8];
#pragma unroll
      for (int k = 0; k < 8; ++k) lf[k] = lds16(kb, fr + k * 64);
      asm volatile("" ::: "memory");
      kt[t] = (f4){0, 0, 0, 0};
#pragma unroll
      for (int k = 0; k < 8; ++k) kt[t] = mfma16(lf[k], rf[k], kt[t]);
    };
#pragma unroll
    for (int a = 0; a < 2; ++a) {
      f2 v0 = G[0], v1 = G[1];
#pragma unroll
      for (int bb = 0; bb < 8; ++bb) { put(8 * a + bb, v0, v1); if (bb < 7) { v0 = cmul(v0, z1[0]); v1 = cmul(v1, z1[1]); } }
      G[0] = cmul(G[0], z128[0]); G[1] = cmul(G[1], z128[1]);
    }
    tile_mma(0);
#pragma unroll
    for (int a = 2; a < 4; ++a) {
      f2 v0 = G[0], v1 = G[1];
#pragma unroll
      for (int bb = 0; bb < 4; ++bb) { put(4 * (a - 2) + bb, v0, v1); if (bb < 3) { v0 = cmul(v0, z2[0]); v1 = cmul(v1, z2[1]); } }
      G[0] = cmul(G[0], z128[0]); G[1] = cmul(G[1], z128[1]);
    }
#pragma unroll
    for (int a = 4; a < 8; ++a) {
      put(8 + 2 * (a - 4), G[0], G[1]);
      put(9 + 2 * (a - 4), cmul(G[0], z4[0]), cmul(G[1], z4[1]));
      G[0] = cmul(G[0], z128[0]); G[1] = cmul(G[1], z128[1]);
    }
    tile_mma(1);
#pragma unroll
    for (int a = 8; a < 17; ++a) {
      put(a - 8, G[0], G[1]);
      if (a < 16) { G[0] = cmul(G[0], z128[0]); G[1] = cmul(G[1], z128[1]); }
    }
    tile_mma(2);
  }
  FENCE();
  {
    LAS u16* k0 = (LAS u16*)kb;
    {
      unsigned kv[4], pv[4];
#pragma unroll
      for (int r = 0; r < 4; ++r) { kv[r] = pk2(kt[2][r], kt[2][r]); pv[r] = __builtin_amdgcn_update_dpp(0u, kv[r], 0x111, 0xf, 0xf, true); }
      if (g < 2) {
#pragma unroll
        for (int r = 0; r < 4; ++r) {
          const int p0 = 128 * (8 + 4 * g + r) + 8 * i16;
          const unsigned last = (i16 & 1) ? ((kv[r] & 0xffffu) | (pv[r] & 0xffff0000u)) : kv[r];
          *(LAS v2u*)(k0 + 2044 - p0) = (v2u){kv[r], kv[r]};
          *(LAS v2u*)(k0 + 2048 - p0) = (v2u){kv[r], last};
        }
      }
      if (lane == 32) *(LAS v2u*)(k0) = (v2u){kv[0], kv[0]};
    }
    asm volatile("" ::: "memory");
    if (g >= 2) {
#pragma unroll
      for (int q = 0; q < 2; ++q) {
        const int p0 = 128 * (4 + 2 * (g - 2) + q) + 8 * i16;
        const float a0 = kt[1][2 * q], a1 = kt[1][2 * q + 1];
        *(LAS unsigned*)(k0 + 2042 - p0) = pk2(a1, a1);
        *(LAS v2u*)(k0 + 2044 - p0) = (v2u){pk2(a1, a0), pk2(a0, a0)};
        *(LAS unsigned*)(k0 + 2048 - p0) = pk2(a0, a0);
      }
    }
    asm volatile("" ::: "memory");
    if (g < 2) {
      const int p0 = 128 * (2 + g) + 8 * i16;
      const unsigned k3 = pk2(kt[1][3], kt[1][0]);
      k0[2041 - p0] = (u16)k3;
      *(LAS unsigned*)(k0 + 2042 - p0) = pk2(kt[1][2], kt[1][2]);
      *(LAS v2u*)(k0 + 2044 - p0) = (v2u){pk2(kt[1][2], kt[1][1]), pk2(kt[1][0], kt[1][0])};
      k0[2048 - p0] = (u16)(k3 >> 16);
    }
    asm volatile("" ::: "memory");
    {
      const int l0 = 128 * (g >> 1) + 8 * i16 + 4 * (g & 1);
      *(LAS v2u*)(k0 + 2044 - l0) = (v2u){pk2(kt[0][3], kt[0][2]), pk2(kt[0][1], kt[0][0])};
    }
    if (lane < 3) *(LAS v4u*)(k0 + 2048 + 8 * lane) = (v4u){0, 0, 0, 0};
    asm volatile("" ::: "memory");
#pragma unroll
    for (int it = 0; it < 5; ++it) {
      const int e = it * 64 + lane;
      if (e < 258) {
        const v4u a = lds16(kb, e * 16);
        const unsigned nx = *(LAS unsigned*)(kb + e * 16 + 16);
        *(LAS v4u*)(kb + 4160 + e * 16) = (v4u){__builtin_amdgcn_alignbyte(a.y, a.x, 2), __builtin_amdgcn_alignbyte(a.z, a.y, 2),
                                                __builtin_amdgcn_alignbyte(a.w, a.z, 2), __builtin_amdgcn_alignbyte(nx, a.w, 2)};
      }
    }
  }
  FENCE();
  ST(2);
  {
    const unsigned fo = WS_FLAG_U + (unsigned)((grp * 4 + (r16 >> 2)) * 16 + i16) * 8;
#pragma unroll 1
    for (unsigned spin = 0; spin < SPIN_CAP; ++spin) {
      asm volatile("" ::: "memory");
      const v2u f = __builtin_amdgcn_raw_buffer_load_b64(rs, fo, 0, 16);
      if (__all(f.x == tagA && f.y == tagB)) break;
      __builtin_amdgcn_s_sleep(2);
    }
    FENCE();
#pragma unroll
    for (int i = 0; i < 4; ++i) ut[i] = __builtin_amdgcn_raw_buffer_load_b128(rs, WS_UT + (unsigned)((grp * 16 + r16) * 16 + 4 * w + i) * 1024 + lane * 16, 0, 16);
#pragma unroll
    for (int i = 0; i < 4; ++i) *(LAS v4u*)(sm + (lane & 3) * WB + 8448 + 480 + (4 * w + i) * 256 + (lane >> 2) * 16) = ut[i];
  }
  BARRIER();
  ST(3);
  f4 acc[8];
#pragma unroll
  for (int t = 0; t < 8; ++t) acc[t] = (f4){0, 0, 0, 0};
  {
    const int ab = (i16 & 1) ? (2031 - i16 + 8 * g) * 2 : 4160 + (2030 - i16 + 8 * g) * 2;
    const int bb = 448 + 32 * i16 + 16 * g;
#pragma unroll 1
    for (int r = 0; r < 8; ++r) {
      v4u A[8], B[8];
#pragma unroll
      for (int q = 0; q < 8; ++q) {
        LAS unsigned* pa = (LAS unsigned*)(kb + ab - 64 * r - 512 * q);
        A[q] = (v4u){pa[0], pa[1], pa[2], pa[3]};
      }
#pragma unroll
      for (int e = 0; e < 8; ++e) B[e] = lds16(ub, bb - 64 * r + 512 * e);
#pragma unroll
      for (int q = 0; q < 8; ++q)
#pragma unroll
        for (int e = 0; e + q < 8; ++e) acc[e + q] = mfma16(A[q], B[e], acc[e + q]);
    }
  }
  FENCE();
  ST(4);
  {
    const float dk = bf2f(rdk);
    const unsigned so = WS_YT + (unsigned)(((grp * 16 + (i16 >> 3)) * 16 + r16) * 4 + w) * 256 + g * 64 + (i16 & 7) * 8;
#pragma unroll
    for (int t = 0; t < 8; ++t) {
      const v2u uv = *(LAS v2u*)(ub + 480 + (256 * t + 16 * i16 + 4 * g) * 2);
      const unsigned y01 = pk2(acc[t][0], acc[t][1]), y23 = pk2(acc[t][2], acc[t][3]);
      const unsigned s01 = pk2(bf2f(uv.x & 0xffffu) * dk, bfhi(uv.x) * dk), s23 = pk2(bf2f(uv.y & 0xffffu) * dk, bfhi(uv.y) * dk);
      const v2u o = {pk2(bf2f(y01 & 0xffffu) + bf2f(s01 & 0xffffu), bfhi(y01) + bfhi(s01)),
                     pk2(bf2f(y23 & 0xffffu) + bf2f(s23 & 0xffffu), bfhi(y23) + bfhi(s23))};
      __builtin_amdgcn_raw_buffer_store_b64(o, rs, so + t * (2 * 16 * 1024), 0, 16);
    }
    asm volatile("s_waitcnt vmcnt(0)" ::: "memory");
    if (lane == 0) __builtin_amdgcn_raw_buffer_store_b64((v2u){tagA, tagB}, rs, WS_FLAG_Y + (unsigned)(grp * 64 + 4 * r16 + w) * 8, 0, 16);
  }
  FENCE();
  ST(5);
  {
    const unsigned fo = WS_FLAG_Y + (unsigned)(grp * 64 + 16 * w + i16) * 8;
#pragma unroll 1
    for (unsigned spin = 0; spin < SPIN_CAP; ++spin) {
      asm volatile("" ::: "memory");
      const v2u f = __builtin_amdgcn_raw_buffer_load_b64(rs, fo, 0, 16);
      if (__all(f.x == tagA && f.y == tagB)) break;
      __builtin_amdgcn_s_sleep(2);
    }
    FENCE();
#pragma unroll
    for (int i = 0; i < 4; ++i) ut[i] = __builtin_amdgcn_raw_buffer_load_b128(rs, WS_YT + (unsigned)((grp * 16 + r16) * 16 + 4 * w + i) * 1024 + lane * 16, 0, 16);
    LAS u16* tp = (LAS u16*)(tile + (32 * (i16 & 3) + 4 * (i16 >> 2)) * 144) + 16 * w + g;
#pragma unroll
    for (int i = 0; i < 4; ++i) {
#pragma unroll
      for (int k = 0; k < 4; ++k) {
        const int row = 16 * (k >> 1) + 2 * (k & 1);
        tp[row * 72 + 4 * i] = (u16)ut[i][k];
        tp[(row + 1) * 72 + 4 * i] = (u16)(ut[i][k] >> 16);
      }
    }
  }
  BARRIER();
  if (r16 == 0 && tid == 0)
    __hip_atomic_store((GAS unsigned*)(p.ws + WS_EPOCH_B + grp * 256), tagA, __ATOMIC_RELAXED, __HIP_MEMORY_SCOPE_AGENT);
#pragma unroll
  for (int i = 0; i < 8; ++i) {
    const int row = 32 * w + 4 * i + g;
    const v2u v = *(LAS v2u*)(tile + row * 144 + i16 * 8);
    __builtin_amdgcn_raw_buffer_store_b128((v4u){v.x << 16, v.x & 0xffff0000u, v.y << 16, v.y & 0xffff0000u}, ro, (unsigned)(128 * r16 + row) * 4608u + (576 + 64 * grp + 4 * i16) * 4u, 0, OUT_AUX);
  }
  ST(6);
}

__global__ void __launch_bounds__(256, 2) ssm_fused(Params p) {
  extern __shared__ __attribute__((aligned(16))) char smem[];
  LAS char* sm = (LAS char*)smem;
  unsigned long long t00 = 0;
  t00 = __builtin_amdgcn_s_memrealtime();
  __builtin_amdgcn_sched_barrier(0);
  unsigned long long tpp = t00;
  const int bid = blockIdx.x;
  unsigned long long pc;
  asm volatile("s_getpc_b64 %0" : "=s"(pc));
  pc &= ~255ull;
  const unsigned part = (((unsigned)bid >> 3) * 4 + (threadIdx.x >> 6)) % 5u;
  unsigned off = part * 8192u + (threadIdx.x & 63) * 128u;
  off = off < CODE_BYTES ? off : 0;
  const unsigned pf = *(const GAS unsigned*)((const GAS char*)pc + off);
  if (bid < NA) role_a(p, sm, bid, tpp);
  else role_b(p, sm, bid - NA, tpp);
  asm volatile("" :: "v"(pf));
  if (bid == PROBE_BID && threadIdx.x < 64) { const unsigned long long until = t00 + 4000 + 4 * (tpp - t00); while (__builtin_amdgcn_s_memrealtime() < until) __builtin_amdgcn_s_sleep(2); }
}

extern "C" void kernel_launch(void* const* d_in, const int* in_sizes, int n_in,
                              void* d_out, int out_size, void* d_ws, size_t ws_size,
                              hipStream_t stream) {
  Params p;
  p.x = (const u16*)d_in[0]; p.Lre = (const u16*)d_in[1]; p.Lim = (const u16*)d_in[2];
  p.Bre = (const u16*)d_in[3]; p.Bim = (const u16*)d_in[4]; p.Cre = (const u16*)d_in[5]; p.Cim = (const u16*)d_in[6];
  p.ldtA = (const u16*)d_in[7]; p.DskA = (const u16*)d_in[8];
  p.u = (const u16*)d_in[9]; p.lA = (const u16*)d_in[10]; p.Aim = (const u16*)d_in[11];
  p.Cr = (const u16*)d_in[12]; p.Ci = (const u16*)d_in[13]; p.ldtB = (const u16*)d_in[14]; p.DskB = (const u16*)d_in[15];
  p.out = (float*)d_out; p.ws = (char*)d_ws;
  (void)hipFuncSetAttribute((const void*)ssm_fused, hipFuncAttributeMaxDynamicSharedMemorySize, LDS_BYTES);
  ssm_fused<<<NA + GRID_B, 256, LDS_BYTES, stream>>>(p);
}
```

```cpp
#include <hip/hip_runtime.h>
#include <hip/hip_bf16.h>
#include <stdint.h>
#include <utility>

typedef unsigned short u16;
typedef unsigned v4u __attribute__((ext_vector_type(4)));
typedef unsigned v2u __attribute__((ext_vector_type(2)));
typedef float v2f __attribute__((ext_vector_type(2)));
struct f2 { float x, y; };
typedef float f4 __attribute__((ext_vector_type(4)));
typedef __bf16 bf2 __attribute__((ext_vector_type(2)));
typedef __bf16 bf8 __attribute__((ext_vector_type(8)));
typedef short s4 __attribute__((ext_vector_type(4)));
#define GAS __attribute__((address_space(1)))
#define LAS __attribute__((address_space(3)))
#define DI __device__ __forceinline__
#define TIDX ((int)__builtin_amdgcn_workitem_id_x())
#define BIDX ((int)__builtin_amdgcn_workgroup_id_x())

constexpr int L = 2048, D = 576, NM = 128;
constexpr int TC = 80, NCH = 26, NS = 9;
constexpr int NA = NCH * NS;
constexpr int NB = 144;
constexpr int LDS_BYTES = 81920;

constexpr unsigned WS_EPOCH_A = 0;
constexpr unsigned WS_EPOCH_B = 256;
constexpr unsigned WS_FLAG_U = 4096;
constexpr unsigned WS_FLAG_C = 4096 + 8192;
constexpr unsigned WS_E = 32768;
constexpr unsigned WS_E_BYTES = NCH * 36 * 256;
constexpr unsigned WS_H = WS_E + 262144;
constexpr unsigned WS_H_BYTES = NCH * 5 * 36 * 1024;
constexpr unsigned WS_UT = WS_H + WS_H_BYTES;
constexpr unsigned WS_UT_BYTES = 9 * 16 * 16 * 1024;
constexpr unsigned WS_YT = WS_UT + WS_UT_BYTES;
constexpr unsigned WS_YT_BYTES = 9 * 16 * 16 * 1024;
constexpr unsigned WS_CT = WS_YT + WS_YT_BYTES;
constexpr unsigned WS_CT_BYTES = 9 * 144 * 1024;
constexpr unsigned WS_TOTAL = WS_CT + WS_CT_BYTES;
#define STEND() do {} while (0)
#define STC(i) do {} while (0)
#define ST(i) do {} while (0)

#define NPOISON 5
#define GRID_B NB
constexpr unsigned CODE_BYTES = 26000;
#define OUT_AUX 17
constexpr unsigned SPIN_CAP = 1u << 17;

#if defined(__HIP_DEVICE_COMPILE__)
#pragma clang attribute push(__attribute__((target("no-packed-fp32-ops"))), apply_to = function)
#endif
template <class F, int... I> __device__ __forceinline__ void sfor_impl(F&& f, std::integer_sequence<int, I...>) { (f(std::integral_constant<int, I>{}), ...); }
template <int N, class F> __device__ __forceinline__ void sfor(F&& f) { sfor_impl(f, std::make_integer_sequence<int, N>{}); }

struct Params {
  const u16 *x, *Lre, *Lim, *Bre, *Bim, *Cre, *Cim, *ldtA, *DskA;
  const u16 *u, *lA, *Aim, *Cr, *Ci, *ldtB, *DskB;
  float* out;
  char* ws;
};

DI float u2f(unsigned v) { return __builtin_bit_cast(float, v); }
DI unsigned f2u(float v) { return __builtin_bit_cast(unsigned, v); }
DI unsigned umax(unsigned a, unsigned b) { return a > b ? a : b; }
DI float bf2f(unsigned v) { return u2f(v << 16); }
DI float bfhi(unsigned v) { return u2f(v & 0xffff0000u); }
DI unsigned pk2(float a, float b) { v2f z = {a, b}; return __builtin_bit_cast(unsigned, __builtin_convertvector(z, bf2)); }
DI float rbf(float f) { return bf2f(pk2(f, 0.f) & 0xffffu); }
DI f2 cmul(f2 a, f2 b) { return (f2){__builtin_fmaf(-a.y, b.y, a.x * b.x), __builtin_fmaf(a.x, b.y, a.y * b.x)}; }
DI f2 csel(bool c, f2 a, f2 b) { return (f2){c ? a.x : b.x, c ? a.y : b.y}; }
DI float fexp(float x) {
  const float c_hi = 1.44269502162933349609375f, c_lo = 1.925963033500011e-8f;
  float t = x * c_hi;
  float e = __builtin_fmaf(x, c_hi, -t);
  e = __builtin_fmaf(x, c_lo, e);
  float r;
  asm volatile("v_exp_f32 %0, %1\n\ts_nop 3" : "=&v"(r) : "v"(t));
  return __builtin_fmaf(r * e, 0.693147180559945f, r);
}
DI float frcp(float x) {
  float r;
  asm volatile("v_rcp_f32 %0, %1\n\ts_nop 3" : "=&v"(r) : "v"(x));
  return __builtin_fmaf(__builtin_fmaf(-x, r, 1.0f), r, r);
}
DI f2 cexp(float re, float im) {
  const float r_hi = 0.15915494f, r_lo = 6.4206382e-9f;
  float m = fexp(re);
  float t = im * r_hi;
  float e = __builtin_fmaf(im, r_hi, -t);
  e = __builtin_fmaf(im, r_lo, e);
  float f = __builtin_amdgcn_fractf(t) + e;
  float cs, sn;
  asm volatile("v_cos_f32 %0, %2\n\tv_sin_f32 %1, %2\n\ts_nop 3" : "=&v"(cs), "=&v"(sn) : "v"(f));
  return (f2){m * cs, m * sn};
}
DI f4 mfma16(v4u a, v4u b, f4 c) {
  return __builtin_amdgcn_mfma_f32_16x16x32_bf16(__builtin_bit_cast(bf8, a), __builtin_bit_cast(bf8, b), c, 0, 0, 0);
}
DI void mfa(f4& acc, v4u a, v4u b) { asm volatile("v_mfma_f32_16x16x32_bf16 %0, %1, %2, %0" : "+v"(acc) : "v"(a), "v"(b)); }
DI void mf0(f4& acc, v4u a, v4u b) { asm volatile("v_mfma_f32_16x16x32_bf16 %0, %1, %2, 0" : "=&v"(acc) : "v"(a), "v"(b)); }
#define MFMA_DRAIN() asm volatile("s_nop 7")
DI float sfma(float a, float b, float c) { float r = __builtin_fmaf(a, b, c); asm volatile("" : "+v"(r)); return r; }
DI v4u ldg16(const void* base, unsigned off, int cst = 0) { return *(const GAS v4u*)((const GAS char*)base + off + cst); }
DI v4u lds16(LAS char* s, unsigned off) { return *(LAS v4u*)(s + off); }
DI v2u ldstr(LAS char* s, unsigned off) {
  return __builtin_bit_cast(v2u, __builtin_amdgcn_ds_read_tr16_b64_v4i16((LAS s4*)(s + off)));
}
#define ST128(...) do { __builtin_amdgcn_raw_buffer_store_b128(__VA_ARGS__); __builtin_amdgcn_sched_barrier(0); asm volatile("s_nop 1"); __builtin_amdgcn_sched_barrier(0); } while (0)
#define FENCE() do { __builtin_amdgcn_sched_barrier(0); asm volatile("" ::: "memory"); } while (0)
#define BARRIER() do { __builtin_amdgcn_sched_barrier(0); asm volatile("s_waitcnt lgkmcnt(0)\n\ts_barrier" ::: "memory"); __builtin_amdgcn_sched_barrier(0); } while (0)

DI float xsum_groups(float v) {
  auto a = __builtin_amdgcn_permlane16_swap(f2u(v), f2u(v), false, false);
  v = u2f(a[0]) + u2f(a[1]);
  auto b = __builtin_amdgcn_permlane32_swap(f2u(v), f2u(v), false, false);
  return u2f(b[0]) + u2f(b[1]);
}
DI bool anyl(bool b) { return __builtin_amdgcn_ballot_w64(b) != 0ull; }
#define PIN(x) asm volatile("" : "+v"(x))
#define PINC(z) asm volatile("" : "+v"((z).x), "+v"((z).y))
DI unsigned or3(unsigned a, unsigned b, unsigned c) { unsigned r = a | b | c; PIN(r); return r; }
DI unsigned tag2(unsigned t) { return (t * 0x9E3779B1u) ^ 0x5bd1e995u; }
DI unsigned ld_epoch(const char* ws, unsigned off) {
  return __hip_atomic_load((const GAS unsigned*)(ws + off), __ATOMIC_RELAXED, __HIP_MEMORY_SCOPE_AGENT);
}

#define SB() __builtin_amdgcn_sched_barrier(0)
template <int X2> DI unsigned cnd(unsigned src, unsigned keep, unsigned long long m) {
  unsigned d;
  register unsigned long long mv asm("vcc") = m;
  if (X2) asm volatile("v_cndmask_b32_dpp %0, %1, %2, vcc quad_perm:[2,3,0,1] row_mask:0xf bank_mask:0xf" : "=v"(d) : "v"(src), "v"(keep), "s"(mv));
  else asm volatile("v_cndmask_b32_dpp %0, %1, %2, vcc quad_perm:[1,0,3,2] row_mask:0xf bank_mask:0xf" : "=v"(d) : "v"(src), "v"(keep), "s"(mv));
  return d;
}
DI float fadd(float a, float b) { float r = a + b; PIN(r); return r; }
DI f4 fadd4(f4 a, f4 b) { return (f4){fadd(a[0], b[0]), fadd(a[1], b[1]), fadd(a[2], b[2]), fadd(a[3], b[3])}; }
#define E_AT 1
#define H_DELAY 0
#define REQ_P 4
#define PRO_N 32
DI void role_a(const Params& p, LAS char* sm, int bid, unsigned long long& tpp) {
  unsigned stv = 0; (void)stv;
  const int tid = TIDX, lane = tid & 63;
  const int w = __builtin_amdgcn_readfirstlane(tid >> 6);
  int id; { const int q = NA / 8, r = NA % 8, xcd = bid & 7, off = bid >> 3; id = (xcd < r ? xcd * (q + 1) : r * (q + 1) + (xcd - r) * q) + off; }
  const int c = id / NS, s = id % NS;
  const int i16 = lane & 15, g = lane >> 4;
  const auto rs = __builtin_amdgcn_make_buffer_rsrc((void*)p.ws, 0, WS_TOTAL, 0x00020000);
  const auto rx = __builtin_amdgcn_make_buffer_rsrc((void*)p.x, 0, L * D * 2, 0x00020000);
  const auto rbr = __builtin_amdgcn_make_buffer_rsrc((void*)p.Bre, 0, D * D * 2, 0x00020000);
  const auto rbi = __builtin_amdgcn_make_buffer_rsrc((void*)p.Bim, 0, D * D * 2, 0x00020000);
  __builtin_amdgcn_s_setprio(2);
  ST(0); STC(14);

  auto kb_of = [&](int ord) { int k = s + 1 + ord; return k >= 9 ? k - 9 : k; };
  const int c0 = lane & 3, c1 = (lane >> 2) & 3;
  const unsigned xv0 = (unsigned)(20 * (lane >> 5) + 10 * ((lane >> 3) & 1) + ((lane >> 4) & 1)) * 1152u + (lane & 7) * 16u;
  const unsigned xva = xv0 + (unsigned)((w & 1) ? 44 : 0) * 1152u, xvb = xv0 + (unsigned)((w & 1) ? 6 : 40) * 1152u;
  int xso[4];
#pragma unroll
  for (int G = 0; G < 4; ++G) xso[G] = c * TC * 1152 + kb_of(2 * G + (w >> 1)) * 128;
  const unsigned brow = (unsigned)(4 * (c1 & 1) + 32 * (c1 >> 1)) * 1152u;
  const int bso = (64 * s + 8 * w) * 1152;
  v4u xs[5][5], bw[5][8];
  unsigned bvo[4];
  auto load_x = [&](int G, int i) {
    if (G < 4) {
      if (i < 4) xs[G][i] = __builtin_amdgcn_raw_buffer_load_b128(rx, (i & 1 ? xvb : xva) + (i >> 1) * 2304, xso[G], 0);
      else xs[G][i] = __builtin_amdgcn_raw_buffer_load_b128(rx, xva, xso[G] + 4608, 0);
    } else {
      int sub = w + 4 * i; sub = sub < 10 ? sub : 9;
      xs[4][i] = __builtin_amdgcn_raw_buffer_load_b128(rx, xv0, (c * TC + 40 * (sub & 1) + 2 * (sub >> 1)) * 1152 + s * 128, 0);
    }
  };
  const char* mb = (const char*)(c0 >> 1 ? p.Bim : p.Bre) + (bso + brow + (unsigned)s * 128u + (4 * (g & 1) + 2 * (g >> 1) + (c0 & 1)) * 16u);
  auto load_b = [&](int G, int j) {
    if (G < 4) bw[G][j] = __builtin_amdgcn_raw_buffer_load_b128(j >> 2 ? rbi : rbr, bvo[G] + (j & 3) * 1152, bso, 0);
    else bw[4][j] = ldg16(mb, 0, j * 1152);
  };
  const int kap = 4 * s + w;
  const unsigned hslot = kap < 32 ? 9 * (kap >> 3) + (kap & 7) : 9 * (kap - 32) + 8;
  const unsigned hst = WS_H + (unsigned)(c * 180 + hslot) * 1024 + lane * 16;
  auto request = [&](auto Q) {
    constexpr int q = Q;
    if constexpr (q < 52) { constexpr int G = q / 13, r = q % 13; if constexpr (r < 5) load_x(G, r); else load_b(G, r - 5); }
    else if constexpr (q < 55) load_x(4, q - 52);
    else if constexpr (q < 59) load_b(4, q - 55);
    else if constexpr (q < 64) ST128((v4u){~0u, ~0u, ~0u, ~0u}, rs, hst, (q - 59) * 36864, 16);
  };
#define REQ(a, n) do { SB(); sfor<n>([&](auto J) { request(std::integral_constant<int, (a) + J>{}); }); SB(); } while (0)
  const int pl = 64 * s + 8 * w + (i16 & 7) + 32 * (i16 >> 3);
  const unsigned rLre = *(const GAS u16*)(p.Lre + pl), rLim = *(const GAS u16*)(p.Lim + pl), rLdt = *(const GAS u16*)(p.ldtA + pl);
  const unsigned ep = ld_epoch(p.ws, WS_EPOCH_A);
  const v2u dsk = *(const GAS v2u*)(p.DskA + 64 * s + 4 * i16);
  REQ(0, 5);
#pragma unroll
  for (int G = 0; G < 4; ++G) { bvo[G] = brow + (unsigned)(g >> 1 ? kb_of(2 * G + 1) : kb_of(2 * G)) * 128u + (4 * (g & 1) + c0) * 16u; PIN(bvo[G]); }
  REQ(5, 8);

  const float dt = rbf(fexp(bf2f(rLdt)));
  const float lre = bf2f(rLre), lim = bf2f(rLim);
  float ar = dt * lre, ai = dt * lim;
  PIN(ar); PIN(ai);
  REQ(13, 2);
  f2 lam = cexp(ar, ai);
  PINC(lam);
  REQ(15, 2);
  REQ(17, 2);
  f2 l10 = cexp(ar * 10.f, ai * 10.f);
  PINC(l10);
  REQ(19, 2);
  f2 l80 = cexp(ar * 80.f, ai * 80.f);
  PINC(l80);
  REQ(21, 2);
  f2 l20 = cmul(l10, l10);
  const f2 l40 = cmul(l20, l20), l160 = cmul(l80, l80);
  f2 w320 = cmul(l160, l160);
  const f2 one = {1.f, 0.f};
  f2 wend = cmul(csel(g & 1, one, l20), csel(g & 2, one, l40));
  PINC(l20); PINC(w320); PINC(wend);
  REQ(23, 3);
  f2 wH = cmul(csel(g & 1, l20, one), csel(g & 2, l40, one));
  f2 wg80 = cmul(csel(g & 1, l80, one), csel(g & 2, l160, one));
  unsigned tagA = ep + 1u;
  unsigned est = g == 0 ? WS_E + (unsigned)(c * 36 + kap) * 256 + i16 * 16 : 0x80000000u;
  PINC(wH); PINC(wg80); PIN(tagA); PIN(est);
  FENCE();
  ST(10);

  const unsigned xw = (lane >> 3) * 128 + (((lane & 7) ^ (lane >> 4)) * 16);
  LAS char* xwb = sm + xw + (w >> 1) * 10240 + (w & 1) * 5120;
  const unsigned fsw = (i16 >> 1) & 3;
  LAS char* rdx[4];
#pragma unroll
  for (int k = 0; k < 4; ++k) rdx[k] = sm + (g >> 1) * 10240 + i16 * 128 + (g & 1) * 64 + ((k ^ fsw) * 16);
  LAS char* rd8 = sm + 71680 + i16 * 128;
  const unsigned k8 = 4 * (g & 1) + 2 * (g >> 1);
  auto store_x = [&](int G, int i) {
    if (G < 4) *(LAS v4u*)(xwb + (G % 3) * 20480 + i * 1024) = xs[G][i];
    else { int sub = w + 4 * i; sub = sub < 10 ? sub : 9; *(LAS v4u*)(sm + 71680 + xw + sub * 1024) = xs[4][i]; }
  };
  v4u bt[8], bf[2][8], xa[2][5];
  unsigned long long qm[4] = {0x5555555555555555ull, 0xAAAAAAAAAAAAAAAAull, 0x3333333333333333ull, 0xCCCCCCCCCCCCCCCCull};
#pragma unroll
  for (int i = 0; i < 4; ++i) asm volatile("" : "+s"(qm[i]));
  auto tr_step = [&](auto GG, auto U) {
    constexpr int G = GG, u = U, NC = G < 4 ? 16 : 8, ph = u / NC, r = u % NC, m = (r >> 3) * 4, d = (r >> 1) & 3, h = r & 1;
    if constexpr (ph == 0) bt[m + 2 * h][d] = cnd<0>(bw[G][m + 2 * h + 1][d], bw[G][m + 2 * h][d], qm[0]);
    if constexpr (ph == 1) bt[m + 2 * h + 1][d] = cnd<0>(bw[G][m + 2 * h][d], bw[G][m + 2 * h + 1][d], qm[1]);
    if constexpr (ph == 2) bf[G & 1][m + h][d] = cnd<1>(bt[m + 2 + h][d], bt[m + h][d], qm[2]);
    if constexpr (ph == 3) bf[G & 1][m + 2 + h][d] = cnd<1>(bt[m + h][d], bt[m + 2 + h][d], qm[3]);
  };
  auto read_a = [&](int ks, int t) {
    if (ks < 16) xa[ks & 1][t] = *(LAS v4u*)(rdx[ks & 3] + ((ks >> 2) % 3) * 20480 + t * 2048);
    else xa[ks & 1][t] = *(LAS v4u*)(rd8 + (((k8 + (ks & 1)) ^ fsw) * 16) + t * 2048);
  };
  f4 aR[5], aI[5];
  v2u cfl;
  unsigned cfo = WS_FLAG_C + (unsigned)((s * 9 + (lane < 8 ? 2 * w + (lane >> 2) : 8)) * 4 + (lane & 3)) * 8;
  PIN(cfo);
  sfor<64>([&](auto U) {
    constexpr int u = U;
    tr_step(std::integral_constant<int, 0>{}, U);
    if constexpr (u % 2 == 1) {
      constexpr int f = u / 2;
      if constexpr (f < 10 && f % 2 == 0) store_x(0, f / 2);
      else if constexpr (f >= 10 && f % 2 == 0 && 26 + (f - 10) / 2 < PRO_N) request(std::integral_constant<int, 26 + (f - 10) / 2>{});
      else asm volatile("s_nop 0");
    }
    SB();
    if constexpr (u == 23) { BARRIER(); }
    if constexpr (u >= 40 && u < 50 && u % 2 == 0) { read_a(0, (u - 40) / 2); SB(); }
  });
  static_assert(PRO_N >= 32 && PRO_N <= 37, "request schedule");
  FENCE();
  ST(1); STC(19);
  sfor<18>([&](auto KS) {
    constexpr int ks = KS, G = ks / 4, k = ks % 4;
    sfor<10>([&](auto GP) {
      constexpr int gp = GP, t = gp >> 1, im = gp & 1, sl = 10 * k + gp;
      if constexpr (k == 3 && gp == 0) BARRIER();
      if constexpr (gp == 0) asm volatile("" :: "v"(xa[ks & 1][4]));
      const v4u bb = G < 4 ? bf[G & 1][4 * im + k] : bf[0][2 * im + k];
      if constexpr (ks == 0) mf0(im ? aI[t] : aR[t], xa[0][t], bb); else mfa(im ? aI[t] : aR[t], xa[ks & 1][t], bb);
      SB();
      if constexpr (gp < 5 && ks < 17) read_a(ks + 1, gp);
      if constexpr (G < 4) {
        constexpr int NT = G < 3 ? 64 : 32;
        constexpr int u0 = G < 3 ? 16 * k + (gp < 4 ? gp : 2 * gp - 4) : sl - 4, cnt = G < 3 && gp >= 4 ? 2 : 1;
        sfor<cnt>([&](auto J) { constexpr int u = u0 + J; if constexpr (u >= 0 && u < NT) tr_step(std::integral_constant<int, G + 1>{}, std::integral_constant<int, u>{}); });
      }
      if constexpr (G < 4 && k < 3 && (gp == 6 || gp == 9) && 2 * k + (gp == 9) < (G < 3 ? 5 : 3)) store_x(G + 1, 2 * k + (gp == 9));
      if constexpr ((10 * ks + gp) % REQ_P == 1) request(std::integral_constant<int, PRO_N + (10 * ks + gp) / REQ_P>{});
      if constexpr (ks == 16 && gp == 6) cfl = __builtin_amdgcn_raw_buffer_load_b64(rs, cfo, 0, 16);
      SB();
    });
    if constexpr (k & 1) STC(20 + ks / 2);
  });
  MFMA_DRAIN();
  FENCE();
  ST(2);

  float bR[2][10], bI[2][10];
#pragma unroll
  for (int j = 0; j < 10; ++j)
#pragma unroll
    for (int h = 0; h < 2; ++h) { bR[h][j] = aR[j >> 1][2 * (j & 1) + h]; bI[h][j] = aI[j >> 1][2 * (j & 1) + h]; }
  const float nly = -lam.y;
  float h9r, h9i, h19r, h19i;
  auto crot = [&](float& r, float& i, float ar_, float ai_, f2 wv, float cr, float ci) {
    const float tr = sfma(ar_, wv.x, cr), ti = sfma(ai_, wv.x, ci);
    r = sfma(-ai_, wv.y, tr); i = sfma(ar_, wv.y, ti);
  };
  {
    float R[2] = {bR[0][0], bR[1][0]}, I[2] = {bI[0][0], bI[1][0]};
#pragma unroll
    for (int j = 1; j < 10; ++j) {
      const float t0 = sfma(R[0], lam.x, bR[0][j]), t1 = sfma(I[0], lam.x, bI[0][j]), t2 = sfma(R[1], lam.x, bR[1][j]), t3 = sfma(I[1], lam.x, bI[1][j]);
      const float n0 = sfma(I[0], nly, t0), n1 = sfma(R[0], lam.y, t1), n2 = sfma(I[1], nly, t2), n3 = sfma(R[1], lam.y, t3);
      R[0] = n0; I[0] = n1; R[1] = n2; I[1] = n3;
    }
    h9r = R[0]; h9i = I[0];
    crot(h19r, h19i, h9r, h9i, l10, R[1], I[1]);
    float er, ei;
    crot(er, ei, h19r, h19i, wend, 0.f, 0.f);
    er = xsum_groups(er); ei = xsum_groups(ei);
    asm volatile("s_waitcnt vmcnt(1)" ::: "memory");
    ST128((v4u){f2u(er), tagA, f2u(ei), tagA}, rs, est, 0, 16);
  }
  FENCE();
  ST(3);
#pragma unroll 1
  for (unsigned spin = 0; spin < SPIN_CAP; ++spin) {
    if (!anyl(cfl.x != tagA || cfl.y != tagA)) break;
    asm volatile("" ::: "memory");
    cfl = __builtin_amdgcn_raw_buffer_load_b64(rs, cfo, 0, 16);
  }
  FENCE();
  v4u cf[9][4];
  const unsigned cld = WS_CT + (unsigned)(s * 4 + w) * 36864 + lane * 16;
  auto load_c = [&](int q) { cf[q >> 2][q & 3] = __builtin_amdgcn_raw_buffer_load_b128(rs, cld + (q & 3) * 1024, (q >> 2) * 4096, 16); };
  const int kq = lane < 8 ? 8 * w + lane : 32 + w;
  unsigned oq = WS_E + (unsigned)(c * 36 + kq) * 256;
  const int cp = c - 1 - g;
  const unsigned oe = WS_E + (unsigned)(cp * 36 + kap) * 256 + i16 * 16;
  unsigned tm[7], om[7];
  PIN(oq);
#pragma unroll
  for (int m = 0; m < 7; ++m) {
    tm[m] = cp - 4 * m >= 0 ? tagA : 0u; om[m] = cp - 4 * m >= 0 ? oe - m * (4 * 36 * 256) : 0x80000000u;
    PIN(tm[m]); PIN(om[m]);
    if (m & 1) load_c(28 + (m >> 1));
    SB();
  }
  float prr, pri;
  {
    auto r16 = __builtin_amdgcn_permlane16_swap(f2u(h19r), f2u(h19r), false, false);
    auto i16s = __builtin_amdgcn_permlane16_swap(f2u(h19i), f2u(h19i), false, false);
    auto r02 = __builtin_amdgcn_permlane32_swap(r16[0], r16[0], false, false);
    auto i02 = __builtin_amdgcn_permlane32_swap(i16s[0], i16s[0], false, false);
    load_c(31); SB();
    auto r13 = __builtin_amdgcn_permlane32_swap(r16[1], r16[1], false, false);
    auto i13 = __builtin_amdgcn_permlane32_swap(i16s[1], i16s[1], false, false);
    const float e0r = u2f(r02[0]), e0i = u2f(i02[0]);
    float p2r, p2i, p3r, p3i;
    crot(p2r, p2i, e0r, e0i, l20, u2f(r13[0]), u2f(i13[0]));
    crot(p3r, p3i, p2r, p2i, l20, u2f(r02[1]), u2f(i02[1]));
    load_c(32); SB();
    prr = g & 2 ? (g & 1 ? p3r : p2r) : (g & 1 ? e0r : 0.f); pri = g & 2 ? (g & 1 ? p3i : p2i) : (g & 1 ? e0i : 0.f);
    PIN(prr); PIN(pri);
  }
#pragma unroll
  for (int q = 33; q < 33 + E_AT; ++q) load_c(q % 36);
  v4u e[7], eq;
  auto load_e = [&]() {
    eq = __builtin_amdgcn_raw_buffer_load_b128(rs, oq, 0, 16);
#pragma unroll
    for (int m = 0; m < 7; ++m) e[m] = __builtin_amdgcn_raw_buffer_load_b128(rs, om[m], 0, 16);
  };
  FENCE();
  load_e();
  BARRIER();
  ST(4);
  LAS char* cls = sm + w * 8192 + lane * 16;
#pragma unroll
  for (int q = 33 + E_AT; q < 36 + 16; ++q) {
    load_c(q % 36);
    if (q >= 44) *(LAS v4u*)(cls + (q - 44) * 1024) = cf[7 + ((q - 44) >> 2)][q & 3];
    SB();
  }
  FENCE();
  float Hr, Hi;
  {
#pragma unroll 1
    for (unsigned spin = 0; spin < SPIN_CAP; ++spin) {
      unsigned bad = (eq.y ^ tagA) | (eq.w ^ tagA);
#pragma unroll
      for (int m = 0; m < 7; ++m) bad = or3(bad, e[m].y ^ tm[m], e[m].w ^ tm[m]);
      if (!anyl(bad != 0)) break;
      asm volatile("" ::: "memory");
      load_e();
    }
    Hr = u2f(e[6].x); Hi = u2f(e[6].z);
#pragma unroll
    for (int m = 5; m >= 0; --m) crot(Hr, Hi, Hr, Hi, w320, u2f(e[m].x), u2f(e[m].z));
    crot(Hr, Hi, Hr, Hi, wg80, 0.f, 0.f);
    Hr = xsum_groups(Hr); Hi = xsum_groups(Hi);
  }
  FENCE();
  ST(5);
  {
    float R[2], I[2];
    crot(R[0], I[0], Hr, Hi, wH, prr, pri);
    crot(R[1], I[1], R[0], I[0], l10, h9r, h9i);
    LAS char* tb = sm + 32768 + w * 5376;
    LAS char* tw = tb + i16 * 168 + 40 * g;
    unsigned pr[10], pi[10];
#pragma unroll
    for (int j = 0; j < 10; ++j) {
      const float t0 = sfma(R[0], lam.x, bR[0][j]), t1 = sfma(I[0], lam.x, bI[0][j]), t2 = sfma(R[1], lam.x, bR[1][j]), t3 = sfma(I[1], lam.x, bI[1][j]);
      const float n0 = sfma(I[0], nly, t0), n1 = sfma(R[0], lam.y, t1), n2 = sfma(I[1], nly, t2), n3 = sfma(R[1], lam.y, t3);
      if (j & 1) {
        pr[j >> 1] = pk2(R[0], n0); pi[j >> 1] = pk2(-I[0], -n1); pr[5 + (j >> 1)] = pk2(R[1], n2); pi[5 + (j >> 1)] = pk2(-I[1], -n3);
      }
      R[0] = n0; I[0] = n1; R[1] = n2; I[1] = n3;
      if (j == 3) { *(LAS v2u*)(tw) = (v2u){pr[0], pr[1]}; *(LAS v2u*)(tw + 16 * 168) = (v2u){pi[0], pi[1]}; }
      if (j == 5) { *(LAS v2u*)(tw + 24) = (v2u){pr[6], pr[7]}; *(LAS v2u*)(tw + 16 * 168 + 24) = (v2u){pi[6], pi[7]}; }
      if (j == 7) { *(LAS v2u*)(tw + 8) = (v2u){pr[2], pr[3]}; *(LAS v2u*)(tw + 16 * 168 + 8) = (v2u){pi[2], pi[3]}; }
      SB();
    }
    *(LAS v2u*)(tw + 16) = (v2u){pr[4], pr[5]}; *(LAS v2u*)(tw + 16 * 168 + 16) = (v2u){pi[4], pi[5]};
    *(LAS v2u*)(tw + 32) = (v2u){pr[8], pr[9]}; *(LAS v2u*)(tw + 16 * 168 + 32) = (v2u){pi[8], pi[9]};
    asm volatile("" ::: "memory");
    const unsigned ra = (8 * g + (i16 >> 2)) * 168 + (i16 & 3) * 8;
    v2u lo[5], hi[5];
#pragma unroll
    for (int t = 4; t >= 0; --t) { lo[t] = ldstr(tb, ra + 32 * t); hi[t] = ldstr(tb, ra + 4 * 168 + 32 * t); }
#pragma unroll
    for (int t = 4; t >= 0; --t) ST128((v4u){lo[t].x, lo[t].y, hi[t].x, hi[t].y}, rs, hst, t * 36864, 16);
  }
  BARRIER();
  ST(6);

  const auto ro = __builtin_amdgcn_make_buffer_rsrc((void*)p.out, 0, L * 4608, 0x00020000);
  const unsigned hld = WS_H + (unsigned)(c * 180 + 9 * w) * 1024 + lane * 16;
  v4u hf[2][9];
  auto load_h = [&](int t, int i) { hf[t & 1][i] = __builtin_amdgcn_raw_buffer_load_b128(rs, hld + (i & 3) * 1024, t * 36864 + (i >> 2) * 4096, 16); };
  auto mx = [&](unsigned m, v4u v) { m = umax(umax(m, v.x), v.y); return umax(umax(m, v.z), v.w); };
  constexpr int HORD[9] = {0, 7, 1, 2, 8, 3, 4, 5, 6};
  v4u cl[2];
  cl[0] = *(LAS v4u*)(cls); cl[1] = *(LAS v4u*)(cls + 1024);
  v2u sk[5];
  {
    v2u xv[5];
#pragma unroll
    for (int t = 0; t < 5; ++t) {
      const int tl = 16 * t + 4 * w + g, gq = tl / 20, rem = tl - 20 * gq, hb = rem >= 10, rr = rem - 10 * hb, rho = 4 * gq + 2 * (rr & 1) + hb;
      xv[t] = *(LAS v2u*)(sm + 71680 + (rr >> 1) * 2048 + rho * 128 + (((i16 >> 1) ^ ((rho >> 1) & 3)) * 16) + (i16 & 1) * 8);
      SB();
    }
    const float dk[4] = {bf2f(dsk.x & 0xffffu), bfhi(dsk.x), bf2f(dsk.y & 0xffffu), bfhi(dsk.y)};
#pragma unroll
    for (int t = 0; t < 5; ++t) {
      float a0 = bf2f(xv[t].x & 0xffffu) * dk[0], a1 = bfhi(xv[t].x) * dk[1], a2 = bf2f(xv[t].y & 0xffffu) * dk[2], a3 = bfhi(xv[t].y) * dk[3];
      PIN(a0); PIN(a1); PIN(a2); PIN(a3);
      sk[t] = (v2u){pk2(a0, a1), pk2(a2, a3)};
      PIN(sk[t]);
      SB();
    }
  }
  FENCE();
  ST(7);
  if (H_DELAY) __builtin_amdgcn_s_sleep(H_DELAY);
#pragma unroll
  for (int i = 0; i < 9; ++i) load_h(0, HORD[i]);
#pragma unroll
  for (int q = 16; q < 28; ++q) load_c(q);
#pragma unroll
  for (int i = 0; i < 6; ++i) load_h(1, HORD[i]);
  FENCE();
#pragma unroll 1
  for (unsigned spin = 0; spin < SPIN_CAP; ++spin) {
    unsigned m = 0;
#pragma unroll
    for (int i = 0; i < 9; ++i) m = mx(m, hf[0][HORD[i]]);
    if (!anyl(m == ~0u)) break;
    asm volatile("" ::: "memory");
#pragma unroll
    for (int i = 0; i < 15; ++i) load_h(i / 9, HORD[i % 9]);
  }
  FENCE();
  ST(8); STC(30);
  LAS char* rw = sm + w * 4352 + i16 * 272 + g * 16;
  LAS char* rr0 = sm + 32768 + (4 * w + g) * 272 + i16 * 16;
  LAS char* rr1 = rr0 + (54272 - 32768);
  const unsigned ov = (unsigned)(4 * w + g) * 4608u + (64 * s + 4 * i16) * 4u;
  f4 y, y2, y3, yk[2], acc[4];
  auto fin = [&](int t, int st) {
    LAS char* rb = t & 1 ? rr1 : rr0;
    if (st == 0) { y = *(LAS f4*)(rb); y2 = *(LAS f4*)(rb + 4352); }
    if (st == 1) { y3 = *(LAS f4*)(rb + 2 * 4352); }
    if (st == 2) { y[0] = fadd(y[0], y2[0]); y[1] = fadd(y[1], y2[1]); }
    if (st == 3) { y[2] = fadd(y[2], y2[2]); y[3] = fadd(y[3], y2[3]); }
    if (st == 4) { y2 = *(LAS f4*)(rb + 3 * 4352); y[0] = fadd(y[0], y3[0]); }
    if (st == 5) { y[1] = fadd(y[1], y3[1]); y[2] = fadd(y[2], y3[2]); }
    if (st == 6) { y[3] = fadd(y[3], y3[3]); }
    if (st == 7) { y[0] = fadd(y[0], bf2f(sk[t].x & 0xffffu)); }
    if (st == 8) { y[1] = fadd(y[1], bfhi(sk[t].x)); }
    if (st == 9) { y[2] = fadd(y[2], bf2f(sk[t].y & 0xffffu)); }
    if (st == 10) { y[3] = fadd(y[3], bfhi(sk[t].y)); }
    if (st == 11) { y[0] = fadd(y[0], y2[0]); y[1] = fadd(y[1], y2[1]); }
    if (st == 12) { y[2] = fadd(y[2], y2[2]); y[3] = fadd(y[3], y2[3]); }
    if (st == 13) { if (t < 2) yk[t] = y; else ST128(__builtin_bit_cast(v4u, y), ro, ov, (c * TC + 16 * t) * 4608, OUT_AUX); }
  };
  auto put = [&](int t, int n) { *(LAS f4*)(rw + (t & 1 ? 54272 : 32768) + n * 64) = acc[n]; };
  sfor<5>([&](auto T) {
    constexpr int t = T;
    unsigned m = 0;
    sfor<36>([&](auto SL) {
      constexpr int sl = SL;
      if constexpr (sl < 24) {
        constexpr int q = sl / 3, r3 = sl % 3;
        if constexpr (r3 < 2) {
          constexpr int R = 2 * q + r3, i = R >> 2, n = R & 3;
          if constexpr (t > 0 && sl == 3) { put(t - 1, 2); SB(); }
          if constexpr (t > 0 && sl == 4) { put(t - 1, 3); SB(); }
          if constexpr (R < 4) mf0(acc[n], cf[i][n], hf[t & 1][i]); else mfa(acc[n], cf[i][n], hf[t & 1][i]);
        } else { constexpr int n = q & 3; mfa(acc[n], cl[q & 1], hf[t & 1][7 + (q >> 2)]); }
      } else { constexpr int n = (sl - 24) / 3, i = 4 + (sl - 24) % 3; mfa(acc[n], cf[i][n], hf[t & 1][i]); }
      SB();
      if constexpr (sl < 24 && sl % 3 == 2 && (sl / 3 + 2 < 8 || t < 4)) { constexpr int q = (sl / 3 + 2) & 7; cl[q & 1] = *(LAS v4u*)(cls + q * 1024); }
      if constexpr (t > 0 && sl == 9) BARRIER();
      if constexpr (t == 1 && sl == 9) {
        if (id == NA - NS && tid == 0) __hip_atomic_store((GAS unsigned*)(p.ws + WS_EPOCH_A), tagA, __ATOMIC_RELAXED, __HIP_MEMORY_SCOPE_AGENT);
      }
      if constexpr (t > 0) {
        if constexpr (sl == 10 || sl == 12) fin(t - 1, (sl - 10) / 2);
        if constexpr (sl == 18 || sl == 19) fin(t - 1, sl - 16);
        if constexpr (sl == 21 || sl == 22) fin(t - 1, sl - 17);
        if constexpr (sl >= 24 && sl < 29) fin(t - 1, sl - 18);
        if constexpr (sl == 29 || sl == 30) fin(t - 1, sl - 18);
        if constexpr (sl == 33) fin(t - 1, 13);
      }
      if constexpr (t + 1 < 5 && (sl == 0 || sl == 3 || sl == 6)) load_h(t + 1, 4 + sl / 3);
      if constexpr (t + 2 < 5) {
        if constexpr (sl == 9) load_h(t + 2, 0);
        if constexpr (sl == 13) load_h(t + 2, 7);
        if constexpr (sl == 16) load_h(t + 2, 1);
        if constexpr (sl == 20) load_h(t + 2, 2);
        if constexpr (sl == 24) load_h(t + 2, 8);
        if constexpr (sl == 28) load_h(t + 2, 3);
      }
      if constexpr (t == 3 && (sl == 13 || sl == 20)) ST128(__builtin_bit_cast(v4u, yk[sl == 20]), ro, ov, (c * TC + 16 * (sl == 20)) * 4608, OUT_AUX);
      if constexpr (sl == 31 || sl == 34) put(t, (sl - 31) / 3);
      if constexpr (t + 1 < 5) {
        if constexpr (sl == 1 || sl == 7 || sl == 11 || sl == 14 || sl == 15 || sl == 17)
          m = mx(m, hf[(t + 1) & 1][HORD[sl == 1 ? 0 : sl == 7 ? 1 : sl == 11 ? 2 : sl == 14 ? 3 : sl == 15 ? 4 : 5]]);
        if constexpr (sl == 32 || sl == 33 || sl == 35) m = mx(m, hf[(t + 1) & 1][HORD[sl == 32 ? 6 : sl == 33 ? 7 : 8]]);
      }
      SB();
    });
    if constexpr (t + 1 < 5) {
#pragma unroll 1
      for (unsigned spin = 0; spin < SPIN_CAP; ++spin) {
        if (!anyl(m == ~0u)) break;
        asm volatile("" ::: "memory");
        m = 0;
#pragma unroll
        for (int i = 0; i < 9; ++i) load_h(t + 1, i);
#pragma unroll
        for (int i = 0; i < 9; ++i) m = mx(m, hf[(t + 1) & 1][i]);
      }
    }
    FENCE();
    STC(31 + t);
  });
  put(4, 2);
  MFMA_DRAIN();
  put(4, 3);
  BARRIER();
  {
    const f4 a0 = *(LAS f4*)(rr0), a1 = *(LAS f4*)(rr0 + 4352), a2 = *(LAS f4*)(rr0 + 2 * 4352), a3 = *(LAS f4*)(rr0 + 3 * 4352);
    y = (a0 + a1) + (a2 + a3) + (f4){bf2f(sk[4].x & 0xffffu), bfhi(sk[4].x), bf2f(sk[4].y & 0xffffu), bfhi(sk[4].y)};
    fin(4, 13);
  }
  ST(9); STC(15); STEND();
}

DI void role_t(const Params& p, LAS char* sm, int un) {
  const int lane = TIDX & 63;
  const int n = __builtin_amdgcn_readfirstlane(TIDX >> 6);
  const int j = un / 9, sb = un % 9, i16 = lane & 15, g = lane >> 4;
  const auto rs = __builtin_amdgcn_make_buffer_rsrc((void*)p.ws, 0, WS_TOTAL, 0x00020000);
  const auto rcr = __builtin_amdgcn_make_buffer_rsrc((void*)p.Cre, 0, D * D * 2, 0x00020000);
  const auto rci = __builtin_amdgcn_make_buffer_rsrc((void*)p.Cim, 0, D * D * 2, 0x00020000);
  const int pl = 64 * sb + lane;
  const unsigned rLre = *(const GAS u16*)(p.Lre + pl), rLim = *(const GAS u16*)(p.Lim + pl), rLdt = *(const GAS u16*)(p.ldtA + pl);
  const unsigned pv0 = (unsigned)(lane >> 3) * 1152u + (lane & 7) * 16u;
  v4u cw[4];
#pragma unroll
  for (int q = 0; q < 4; ++q) cw[q] = __builtin_amdgcn_raw_buffer_load_b128(q & 2 ? rci : rcr, pv0, (64 * j + 16 * n + 8 * (q & 1)) * 1152 + sb * 128, 0);
  const unsigned tagA = ld_epoch(p.ws, WS_EPOCH_A) + 1u;
  FENCE();
  const float dt = rbf(fexp(bf2f(rLdt)));
  const float lre = bf2f(rLre), lim = bf2f(rLim);
  const f2 lam = cexp(dt * lre, dt * lim);
  const float nr = lam.x - 1.0f, ni = lam.y, den = 2.0f * frcp(lre * lre + lim * lim);
  LAS char* wb = sm + n * 8192;
  *(LAS v2f*)(wb + 4096 + lane * 8) = (v2f){(nr * lre + ni * lim) * den, (ni * lre - nr * lim) * den};
  asm volatile("" ::: "memory");
  f4 co[4];
#pragma unroll
  for (int q = 0; q < 4; ++q) co[q] = *(LAS f4*)(wb + 4096 + (lane & 7) * 64 + q * 16);
  LAS char* stw = wb + (lane >> 3) * 128 + (((lane & 7) ^ (lane >> 4)) * 16);
#pragma unroll
  for (int hf = 0; hf < 2; ++hf) {
    v4u orr, oi;
#pragma unroll
    for (int q = 0; q < 4; ++q) {
      const float r0 = bf2f(cw[hf][q] & 0xffffu), r1 = bfhi(cw[hf][q]), i0 = bf2f(cw[2 + hf][q] & 0xffffu), i1 = bfhi(cw[2 + hf][q]);
      orr[q] = pk2(r0 * co[q][0] - i0 * co[q][1], r1 * co[q][2] - i1 * co[q][3]);
      oi[q] = pk2(r0 * co[q][1] + i0 * co[q][0], r1 * co[q][3] + i1 * co[q][2]);
    }
    *(LAS v4u*)(stw + hf * 1024) = orr;
    *(LAS v4u*)(stw + 2048 + hf * 1024) = oi;
  }
  asm volatile("" ::: "memory");
  LAS char* crd = wb + (g >> 1) * 2048 + i16 * 128 + (g & 1) * 64;
  v4u f[4];
#pragma unroll
  for (int k = 0; k < 4; ++k) f[k] = *(LAS v4u*)(crd + ((k ^ ((i16 >> 1) & 3)) * 16));
#pragma unroll
  for (int k = 0; k < 4; ++k) {
    const int so = sb < 8 ? ((j * 4 + (sb >> 1)) * 36 + (4 * (sb & 1) + k) * 4 + n) * 1024 : ((j * 4 + k) * 36 + 32 + n) * 1024;
    ST128(f[k], rs, WS_CT + lane * 16, so, 16);
  }
  asm volatile("s_waitcnt vmcnt(0)" ::: "memory");
  if (lane == 0) __builtin_amdgcn_raw_buffer_store_b64((v2u){tagA, tagA}, rs, WS_FLAG_C + (unsigned)(un * 4 + n) * 8, 0, 16);
}

DI void role_b(const Params& p, LAS char* sm, int b, unsigned long long& tpp) {
  unsigned stv = 0; (void)stv;
  const int tid = TIDX, lane = tid & 63;
  const int w = __builtin_amdgcn_readfirstlane(tid >> 6);
  const int grp = b >> 4, r16 = b & 15, ch = 64 * grp + 4 * r16 + w;
  const int i16 = lane & 15, g = lane >> 4;
  const auto rs = __builtin_amdgcn_make_buffer_rsrc((void*)p.ws, 0, WS_TOTAL, 0x00020000);
  ST(0);
  constexpr unsigned WB = 13312;
  sm += 128;
  LAS char* kb = sm + w * WB;
  LAS char* ub = kb + 8448;
  LAS char* tile = sm + 4 * WB;

  const unsigned rA = *(const GAS unsigned*)(p.lA + ch * NM + 2 * lane), rI = *(const GAS unsigned*)(p.Aim + ch * NM + 2 * lane);
  const unsigned rCr = *(const GAS unsigned*)(p.Cr + ch * NM + 2 * lane), rCi = *(const GAS unsigned*)(p.Ci + ch * NM + 2 * lane);
  const unsigned rdt = *(const GAS u16*)(p.ldtB + ch), rdk = *(const GAS u16*)(p.DskB + ch);
  const unsigned ep = ld_epoch(p.ws, WS_EPOCH_B + grp * 256);
  v4u ut[4];
#pragma unroll
  for (int i = 0; i < 4; ++i) ut[i] = ldg16(p.u, (unsigned)(128 * r16 + 32 * w + 8 * i + (lane >> 3)) * 1152u + grp * 128u + (lane & 7) * 16u);
#pragma unroll
  for (int i = 0; i < 4; ++i)
    ST128((v4u){~0u, ~0u, ~0u, ~0u}, rs, WS_YT + (unsigned)((grp * 16 + 4 * w) * 16 + r16) * 1024 + lane * 16, i * 16384, 16);
  FENCE();
  if (lane < 30) *(LAS v4u*)(ub + lane * 16) = (v4u){0, 0, 0, 0};

  f4 kt[3];
  unsigned tagA;
  {
    const float dt = rbf(fexp(bf2f(rdt)));
    const unsigned ea = pk2(fexp(bf2f(rA & 0xffffu)), fexp(bfhi(rA)));
    float ar[2] = {-bf2f(ea & 0xffffu) * dt, -bfhi(ea) * dt};
    float ai[2] = {bf2f(rI & 0xffffu) * dt, bfhi(rI) * dt};
    f2 G[2] = {(f2){2.f * (bf2f(rCr & 0xffffu) * dt), 2.f * (bf2f(rCi & 0xffffu) * dt)}, (f2){2.f * (bfhi(rCr) * dt), 2.f * (bfhi(rCi) * dt)}};
    f2 z1[2], z2[2], z4[2], z8[2], z128[2];
#pragma unroll
    for (int m = 0; m < 2; ++m) {
      z1[m] = cexp(ar[m], ai[m]); z8[m] = cexp(8.f * ar[m], 8.f * ai[m]); z128[m] = cexp(128.f * ar[m], 128.f * ai[m]);
      z2[m] = cmul(z1[m], z1[m]); z4[m] = cmul(z2[m], z2[m]);
    }
    auto put = [&](int row, f2 a, f2 b2) { *(LAS v2u*)(kb + row * 528 + lane * 8) = (v2u){pk2(a.x, a.y), pk2(b2.x, b2.y)}; };
    const unsigned fr = i16 * 528 + g * 16;
    v4u rf[8];
    {
      f2 r0 = {1.f, 0.f}, r1 = {1.f, 0.f};
      const f2 c0 = {z8[0].x, -z8[0].y}, c1 = {z8[1].x, -z8[1].y};
#pragma unroll
      for (int j = 0; j < 16; ++j) { put(j, r0, r1); if (j < 15) { r0 = cmul(r0, c0); r1 = cmul(r1, c1); } }
      asm volatile("" ::: "memory");
#pragma unroll
      for (int k = 0; k < 8; ++k) rf[k] = lds16(kb, fr + k * 64);
      asm volatile("" ::: "memory");
    }
    FENCE();
    ST(7);
    tagA = ep + 1u;
#pragma unroll
    for (int i = 0; i < 4; ++i) *(LAS v4u*)(tile + (32 * w + 8 * i + (lane >> 3)) * 144 + (lane & 7) * 16) = ut[i];
    BARRIER();
    {
      const unsigned ra = (8 * g + (i16 >> 2)) * 144 + 32 * w + (i16 & 3) * 8;
      const unsigned so = WS_UT + (unsigned)((grp * 16 + 4 * w + (i16 >> 2)) * 16 + r16) * 1024 + g * 64 + (i16 & 3) * 16;
#pragma unroll
      for (int it = 0; it < 4; ++it) {
        const v2u lo = ldstr(tile, ra + it * 32 * 144), hi = ldstr(tile, ra + (it * 32 + 4) * 144);
        ST128((v4u){lo.x, lo.y, hi.x, hi.y}, rs, so + it * 256, 0, 16);
      }
    }
    FENCE();
    ST(1);
    auto tile_mma = [&](int t) {
      asm volatile("" ::: "memory");
      v4u lf[8];
#pragma unroll
      for (int k = 0; k < 8; ++k) lf[k] = lds16(kb, fr + k * 64);
      asm volatile("" ::: "memory");
      kt[t] = (f4){0, 0, 0, 0};
#pragma unroll
      for (int k = 0; k < 8; ++k) kt[t] = mfma16(lf[k], rf[k], kt[t]);
    };
#pragma unroll
    for (int a = 0; a < 2; ++a) {
      f2 v0 = G[0], v1 = G[1];
#pragma unroll
      for (int bb = 0; bb < 8; ++bb) { put(8 * a + bb, v0, v1); if (bb < 7) { v0 = cmul(v0, z1[0]); v1 = cmul(v1, z1[1]); } }
      G[0] = cmul(G[0], z128[0]); G[1] = cmul(G[1], z128[1]);
    }
    FENCE();
    asm volatile("s_waitcnt vmcnt(0)" ::: "memory");
    if (lane == 0) __builtin_amdgcn_raw_buffer_store_b64((v2u){tagA, tagA}, rs, WS_FLAG_U + (unsigned)((grp * 4 + w) * 16 + r16) * 8, 0, 16);
    FENCE();
    tile_mma(0);
#pragma unroll
    for (int a = 2; a < 4; ++a) {
      f2 v0 = G[0], v1 = G[1];
#pragma unroll
      for (int bb = 0; bb < 4; ++bb) { put(4 * (a - 2) + bb, v0, v1); if (bb < 3) { v0 = cmul(v0, z2[0]); v1 = cmul(v1, z2[1]); } }
      G[0] = cmul(G[0], z128[0]); G[1] = cmul(G[1], z128[1]);
    }
#pragma unroll
    for (int a = 4; a < 8; ++a) {
      put(8 + 2 * (a - 4), G[0], G[1]);
      put(9 + 2 * (a - 4), cmul(G[0], z4[0]), cmul(G[1], z4[1]));
      G[0] = cmul(G[0], z128[0]); G[1] = cmul(G[1], z128[1]);
    }
    tile_mma(1);
#pragma unroll
    for (int a = 8; a < 17; ++a) {
      put(a - 8, G[0], G[1]);
      if (a < 16) { G[0] = cmul(G[0], z128[0]); G[1] = cmul(G[1], z128[1]); }
    }
    tile_mma(2);
  }
  FENCE();
  ST(8);
  const unsigned fo = WS_FLAG_U + (unsigned)((grp * 4 + (r16 >> 2)) * 16 + i16) * 8;
  v2u fl = __builtin_amdgcn_raw_buffer_load_b64(rs, fo, 0, 16);
  FENCE();
  {
    LAS u16* k0 = (LAS u16*)kb;
    {
      unsigned kv[4], pv[4];
#pragma unroll
      for (int r = 0; r < 4; ++r) { kv[r] = pk2(kt[2][r], kt[2][r]); pv[r] = __builtin_amdgcn_update_dpp(0u, kv[r], 0x111, 0xf, 0xf, true); }
      if (g < 2) {
#pragma unroll
        for (int r = 0; r < 4; ++r) {
          const int p0 = 128 * (8 + 4 * g + r) + 8 * i16;
          const unsigned last = (i16 & 1) ? ((kv[r] & 0xffffu) | (pv[r] & 0xffff0000u)) : kv[r];
          *(LAS v2u*)(k0 + 2044 - p0) = (v2u){kv[r], kv[r]};
          *(LAS v2u*)(k0 + 2048 - p0) = (v2u){kv[r], last};
        }
      }
      if (lane == 32) *(LAS v2u*)(k0) = (v2u){kv[0], kv[0]};
    }
    asm volatile("" ::: "memory");
    if (g >= 2) {
#pragma unroll
      for (int q = 0; q < 2; ++q) {
        const int p0 = 128 * (4 + 2 * (g - 2) + q) + 8 * i16;
        const float a0 = kt[1][2 * q], a1 = kt[1][2 * q + 1];
        *(LAS unsigned*)(k0 + 2042 - p0) = pk2(a1, a1);
        *(LAS v2u*)(k0 + 2044 - p0) = (v2u){pk2(a1, a0), pk2(a0, a0)};
        *(LAS unsigned*)(k0 + 2048 - p0) = pk2(a0, a0);
      }
    }
    asm volatile("" ::: "memory");
    if (g < 2) {
      const int p0 = 128 * (2 + g) + 8 * i16;
      const unsigned k3 = pk2(kt[1][3], kt[1][0]);
      k0[2041 - p0] = (u16)k3;
      *(LAS unsigned*)(k0 + 2042 - p0) = pk2(kt[1][2], kt[1][2]);
      *(LAS v2u*)(k0 + 2044 - p0) = (v2u){pk2(kt[1][2], kt[1][1]), pk2(kt[1][0], kt[1][0])};
      k0[2048 - p0] = (u16)(k3 >> 16);
    }
    asm volatile("" ::: "memory");
    {
      const int l0 = 128 * (g >> 1) + 8 * i16 + 4 * (g & 1);
      *(LAS v2u*)(k0 + 2044 - l0) = (v2u){pk2(kt[0][3], kt[0][2]), pk2(kt[0][1], kt[0][0])};
    }
    if (lane < 3) *(LAS v4u*)(k0 + 2048 + 8 * lane) = (v4u){0, 0, 0, 0};
  }
  FENCE();
#pragma unroll 1
  for (unsigned spin = 0; spin < SPIN_CAP; ++spin) {
    if (!anyl(fl.x != tagA || fl.y != tagA)) break;
    __builtin_amdgcn_s_sleep(1);
    asm volatile("" ::: "memory");
    fl = __builtin_amdgcn_raw_buffer_load_b64(rs, fo, 0, 16);
  }
  FENCE();
#pragma unroll
  for (int i = 0; i < 4; ++i) ut[i] = __builtin_amdgcn_raw_buffer_load_b128(rs, WS_UT + (unsigned)((grp * 16 + r16) * 16 + 4 * w) * 1024 + lane * 16, i * 1024, 16);
  FENCE();
  {
    v4u a[4]; unsigned nx[4];
#pragma unroll
    for (int it = 0; it < 4; ++it) { a[it] = lds16(kb, lane * 16 + it * 1024); nx[it] = *(LAS unsigned*)(kb + lane * 16 + it * 1024 + 16); }
#pragma unroll
    for (int it = 0; it < 4; ++it)
      *(LAS v4u*)(kb + 4160 + lane * 16 + it * 1024) = (v4u){__builtin_amdgcn_alignbyte(a[it].y, a[it].x, 2), __builtin_amdgcn_alignbyte(a[it].z, a[it].y, 2),
                                                          __builtin_amdgcn_alignbyte(a[it].w, a[it].z, 2), __builtin_amdgcn_alignbyte(nx[it], a[it].w, 2)};
    if (lane < 2) *(LAS v4u*)(kb + 4160 + 4096 + lane * 16) = (v4u){0, 0, 0, 0};
  }
  FENCE();
  ST(2);
#pragma unroll
  for (int i = 0; i < 4; ++i) *(LAS v4u*)(sm + (lane & 3) * WB + 8448 + 480 + (4 * w + i) * 256 + (lane >> 2) * 16) = ut[i];
  BARRIER();
  ST(3);
  f4 acc[8];
#pragma unroll
  for (int t = 0; t < 8; ++t) { acc[t] = (f4){0, 0, 0, 0}; PIN(acc[t]); }
  {
    const int ab = (i16 & 1) ? (2031 - i16 + 8 * g) * 2 : 4160 + (2030 - i16 + 8 * g) * 2;
    const int bb = 448 + 32 * i16 + 16 * g;
    v4u A[2][8], B[2][8];
    unsigned pa[4];
#pragma unroll
    for (int k = 0; k < 4; ++k) { pa[k] = (unsigned)(size_t)(kb + ab - 128 - 1024 * k - 512); PIN(pa[k]); }
    unsigned pb = (unsigned)(size_t)(ub + bb - 128);
    PIN(pb);
    auto rdA = [&](int buf, int q, int half, int off) {
      LAS unsigned* x = (LAS unsigned*)(size_t)(pa[q >> 1] + off + (q & 1 ? 0 : 512) + 8 * half);
      A[buf][q][2 * half] = x[0]; A[buf][q][2 * half + 1] = x[1];
    };
    auto rdB = [&](int buf, int e, int off) { B[buf][e] = *(LAS v4u*)(size_t)(pb + off + 512 * e); };
#pragma unroll
    for (int q = 0; q < 8; ++q) { rdB(0, q, 128); rdA(0, q, 0, 128); rdA(0, q, 1, 128); }
    FENCE();
#pragma unroll 1
    for (int r2 = 0; r2 < 4; ++r2) {
#pragma unroll
      for (int hh = 0; hh < 2; ++hh) {
        int n = 0;
#pragma unroll
        for (int q = 0; q < 8; ++q)
#pragma unroll
          for (int e = 0; e + q < 8; ++e) {
            mfa(acc[e + q], A[hh][q], B[hh][e]);
            SB();
            if (n < 2) rdA(hh ^ 1, 0, n, hh ? 0 : 64);
            else if (n < 10) rdB(hh ^ 1, n - 2, hh ? 0 : 64);
            else if (n < 24) rdA(hh ^ 1, 1 + ((n - 10) >> 1), n & 1, hh ? 0 : 64);
            if (hh == 1 && n >= 28 && n < 32) { pa[n - 28] -= 128; PIN(pa[n - 28]); }
            if (hh == 1 && n == 32) { pb -= 128; PIN(pb); }
            SB();
            ++n;
          }
      }
    }
  }
  MFMA_DRAIN();
  FENCE();
  ST(4);
  {
    const float dk = bf2f(rdk);
    const unsigned so = WS_YT + (unsigned)(((grp * 16 + (i16 >> 3)) * 16 + r16) * 4 + w) * 256 + g * 64 + (i16 & 7) * 8;
    v2u uv[8];
#pragma unroll
    for (int t = 0; t < 8; ++t) uv[t] = *(LAS v2u*)(ub + 480 + (256 * t + 16 * i16 + 4 * g) * 2);
#pragma unroll
    for (int t = 0; t < 8; ++t) {
      const unsigned y01 = pk2(acc[t][0], acc[t][1]), y23 = pk2(acc[t][2], acc[t][3]);
      const unsigned s01 = pk2(bf2f(uv[t].x & 0xffffu) * dk, bfhi(uv[t].x) * dk), s23 = pk2(bf2f(uv[t].y & 0xffffu) * dk, bfhi(uv[t].y) * dk);
      const v2u o = {pk2(bf2f(y01 & 0xffffu) + bf2f(s01 & 0xffffu), bfhi(y01) + bfhi(s01)),
                     pk2(bf2f(y23 & 0xffffu) + bf2f(s23 & 0xffffu), bfhi(y23) + bfhi(s23))};
      __builtin_amdgcn_raw_buffer_store_b64(o, rs, so, t * (2 * 16 * 1024), 16);
    }
  }
  FENCE();
  ST(5);
  {
#pragma unroll 1
    for (unsigned spin = 0; spin < SPIN_CAP; ++spin) {
      asm volatile("" ::: "memory");
      unsigned m = 0;
#pragma unroll
      for (int i = 0; i < 4; ++i) ut[i] = __builtin_amdgcn_raw_buffer_load_b128(rs, WS_YT + (unsigned)((grp * 16 + r16) * 16 + 4 * w) * 1024 + lane * 16, i * 1024, 16);
#pragma unroll
      for (int i = 0; i < 4; ++i) { m = umax(umax(m, ut[i].x), ut[i].y); m = umax(umax(m, ut[i].z), ut[i].w); }
      if (!anyl(m == ~0u)) break;
      __builtin_amdgcn_s_sleep(2);
    }
    FENCE();
    ST(9);
    LAS u16* tp = (LAS u16*)(tile + (32 * (i16 & 3) + 4 * (i16 >> 2)) * 136 + (i16 & 3) * 8) + 16 * w + g;
#pragma unroll
    for (int i = 0; i < 4; ++i) {
#pragma unroll
      for (int k = 0; k < 4; ++k) {
        const int row = 16 * (k >> 1) + 2 * (k & 1);
        tp[row * 68 + 4 * i] = (u16)ut[i][k];
        tp[(row + 1) * 68 + 4 * i] = (u16)(ut[i][k] >> 16);
      }
    }
  }
  BARRIER();
  if (r16 == 0 && tid == 0)
    __hip_atomic_store((GAS unsigned*)(p.ws + WS_EPOCH_B + grp * 256), tagA, __ATOMIC_RELAXED, __HIP_MEMORY_SCOPE_AGENT);
  const auto ro = __builtin_amdgcn_make_buffer_rsrc((void*)p.out, 0, L * 4608, 0x00020000);
  v2u v[8];
#pragma unroll
  for (int i = 0; i < 8; ++i) v[i] = *(LAS v2u*)(tile + (32 * w + 4 * i + g) * 136 + w * 8 + i16 * 8);
#pragma unroll
  for (int i = 0; i < 8; ++i)
    ST128((v4u){v[i].x << 16, v[i].x & 0xffff0000u, v[i].y << 16, v[i].y & 0xffff0000u}, ro,
                                           (unsigned)g * 4608u + (576 + 64 * grp + 4 * i16) * 4u, (128 * r16 + 32 * w + 4 * i) * 4608, OUT_AUX);
  ST(6); STEND();
}

__global__ void __launch_bounds__(256, 2) ssm_fused(Params p) {
  extern __shared__ __attribute__((aligned(16))) char smem[];
  LAS char* sm = (LAS char*)smem;
  unsigned long long t00 = 0;
  unsigned long long tpp = t00;
  const int bid = BIDX;
  if (__builtin_expect(bid < NA, 1)) {
    role_a(p, sm, bid, tpp);
  } else if (bid < NA + NB) {
    if (GRID_B) role_b(p, sm, bid - NA, tpp);
  } else {
    role_t(p, sm, bid - NA - NB);
  }
}

#if defined(__HIP_DEVICE_COMPILE__)
#pragma clang attribute pop
#endif

extern "C" void kernel_launch(void* const* d_in, const int* in_sizes, int n_in,
                              void* d_out, int out_size, void* d_ws, size_t ws_size,
                              hipStream_t stream) {
  Params p;
  p.x = (const u16*)d_in[0]; p.Lre = (const u16*)d_in[1]; p.Lim = (const u16*)d_in[2];
  p.Bre = (const u16*)d_in[3]; p.Bim = (const u16*)d_in[4]; p.Cre = (const u16*)d_in[5]; p.Cim = (const u16*)d_in[6];
  p.ldtA = (const u16*)d_in[7]; p.DskA = (const u16*)d_in[8];
  p.u = (const u16*)d_in[9]; p.lA = (const u16*)d_in[10]; p.Aim = (const u16*)d_in[11];
  p.Cr = (const u16*)d_in[12]; p.Ci = (const u16*)d_in[13]; p.ldtB = (const u16*)d_in[14]; p.DskB = (const u16*)d_in[15];
  p.out = (float*)d_out; p.ws = (char*)d_ws;
  (void)hipFuncSetAttribute((const void*)ssm_fused, hipFuncAttributeMaxDynamicSharedMemorySize, LDS_BYTES);
  ssm_fused<<<NA + NB + 81, 256, LDS_BYTES, stream>>>(p);
}
```
